```python
import torch

def _softmax(x, axis=-1):
    return torch.softmax(x, dim=axis)
def _gelu(x):
    return torch.nn.functional.gelu(x, approximate="tanh")
def _silu(x):
    return torch.nn.functional.silu(x)
def _sigmoid(x):
    return torch.sigmoid(x)
def _layernorm(x, axis=-1, eps=1e-5):
    m = x.mean(dim=axis, keepdim=True)
    v = x.var(dim=axis, keepdim=True, unbiased=False)
    return (x - m) / torch.sqrt(v + eps)
def _scatter_add(tgt, idx, src, axis=0):
    return tgt.clone().index_add_(axis, idx, src)
def _segment_sum(src, seg, n):
    out = torch.zeros((n,) + src.shape[1:], dtype=src.dtype)
    return out.index_add_(0, seg, src)
def _pad(x, pw):
    flat = tuple(v for pair in reversed(pw) for v in pair)
    return torch.nn.functional.pad(x, flat)

kernel_name = 'interleave_layers__sliding_block_cached__dense_activation_mish__decode'

def setup_inputs():

    _w = (lambda a: (lambda t: t.to(torch.bfloat16) if t.is_floating_point() else t)(torch.as_tensor(a)))
    def _A_setup():
        B, Sq, D = 1, 1, 640
        Hq, Hkv, Sk, Dh = 32, 32, 8192, 192
        assert Hq % Hkv == 0, (Hq, Hkv)
        return {
            "x": _w((lambda *s: torch.randn(*s, dtype=torch.bfloat16) * 0.1)(B, Sq, D)),
            "k_cache": _w((lambda *s: torch.randn(*s, dtype=torch.bfloat16) * 0.8)(B, Hkv, Sk, Dh)),
            "v_cache": _w((lambda *s: torch.randn(*s, dtype=torch.bfloat16) * 3.2)(B, Hkv, Sk, Dh)),
            "wq": _w((lambda *s: torch.randn(*s, dtype=torch.bfloat16) * 0.8)(D, Hq * Dh) * (D ** -0.5)),
            "wo": _w((lambda *s: torch.randn(*s, dtype=torch.bfloat16) * 3.2)(Hq * Dh, D) * ((Hq * Dh) ** -0.5)),
        }
    _A_in = _A_setup()

    def _B_setup():
        B, S, D, F = 1, 1, 640, 1536
        return {
            "x": _w((lambda *s: torch.randn(*s, dtype=torch.bfloat16) * 0.1)(B, S, D)),
            "w1": _w((lambda *s: torch.randn(*s, dtype=torch.bfloat16) * 1.6)(D, F) * (D ** -0.5)),
            "b1": _w((lambda *s: torch.randn(*s, dtype=torch.bfloat16) * 0.4)(F)),
            "w2": _w((lambda *s: torch.randn(*s, dtype=torch.bfloat16) * 0.4)(F, D) * (F ** -0.5)),
        }
    _B_in = _B_setup()

    return {
        "A_x": _A_in["x"],
        "A_k_cache": _A_in["k_cache"],
        "A_v_cache": _A_in["v_cache"],
        "A_wq": _A_in["wq"],
        "A_wo": _A_in["wo"],
        "B_x": _B_in["x"],
        "B_w1": _B_in["w1"],
        "B_b1": _B_in["b1"],
        "B_w2": _B_in["w2"],
    }

def reference(A_x, A_k_cache, A_v_cache, A_wq, A_wo, B_x, B_w1, B_b1, B_w2):
    def _A_ref(x, k_cache, v_cache, wq, wo):
        B, Sq, D = x.shape
        Hq, Dh = 32, 192
        Hkv, Sk = k_cache.shape[1], k_cache.shape[2]
        W = min(4096, max(256, Sk // 4))
        rep = Hq // Hkv
        q = (lambda x, *d: x.permute(*d))((x @ wq).reshape(B, Sq, Hq, Dh), 0, 2, 1, 3)
        k = (lambda x, n, axis: torch.repeat_interleave(x, n, dim=axis))(k_cache[:, :, Sk - W:, :], rep, axis=1)
        v = (lambda x, n, axis: torch.repeat_interleave(x, n, dim=axis))(v_cache[:, :, Sk - W:, :], rep, axis=1)
        scores = torch.einsum("bhqd,bhkd->bhqk", q, k) * (Dh ** -0.5)
        p = _softmax(scores, axis=-1)
        o = torch.einsum("bhqk,bhkd->bhqd", p, v)
        return (lambda x, *d: x.permute(*d))(o, 0, 2, 1, 3).reshape(B, Sq, Hq * Dh) @ wo

    def _B_ref(x, w1, b1, w2):
        h = torch.einsum("bsd,df->bsf", x, w1) + b1
        sp = torch.log1p(torch.exp(-torch.abs(h))) + torch.where(h > 0, h, torch.zeros_like(h))
        act = h * torch.tanh(sp)
        return torch.einsum("bsf,fd->bsd", act, w2)

    x = A_x
    x = x + _A_ref(_layernorm(x, axis=-1), A_k_cache, A_v_cache, A_wq, A_wo)
    x = x + _B_ref(_layernorm(x, axis=-1), B_w1, B_b1, B_w2)
    x = x + _A_ref(_layernorm(x, axis=-1), A_k_cache, A_v_cache, A_wq, A_wo)
    x = x + _B_ref(_layernorm(x, axis=-1), B_w1, B_b1, B_w2)
    return x
```

```cpp
#include <hip/hip_runtime.h>
#include <hip/hip_bf16.h>
typedef unsigned short u16;
__device__ __forceinline__ float bf2f(u16 b){ return __uint_as_float(((unsigned)b)<<16); }
constexpr int D=640, HQ=32, SK=8192, DH=192, W=2048, F=1536, NS=8, KPS=W/NS;

__device__ void ln_to_lds(const float* xs, float* xn, float* red){
  int t=threadIdx.x; float s=0;
  for(int i=t;i<D;i+=blockDim.x) s+=xs[i];
  red[t]=s; __syncthreads();
  for(int o=blockDim.x/2;o>0;o>>=1){ if(t<o) red[t]+=red[t+o]; __syncthreads(); }
  float m=red[0]/D; __syncthreads();
  s=0; for(int i=t;i<D;i+=blockDim.x){ float d=xs[i]-m; s+=d*d; }
  red[t]=s; __syncthreads();
  for(int o=blockDim.x/2;o>0;o>>=1){ if(t<o) red[t]+=red[t+o]; __syncthreads(); }
  float v=red[0]/D; __syncthreads();
  float r=1.0f/sqrtf(v+1e-5f);
  for(int i=t;i<D;i+=blockDim.x) xn[i]=(xs[i]-m)*r;
  __syncthreads();
}
__global__ void k_init(const u16* x, float* xf){ int t=threadIdx.x+blockIdx.x*blockDim.x; if(t<D) xf[t]=bf2f(x[t]); }
__global__ void k_q(const float* xf, const u16* wq, float* q){
  __shared__ float xs[D], xn[D], red[256];
  int t=threadIdx.x; for(int i=t;i<D;i+=256) xs[i]=xf[i]; __syncthreads();
  ln_to_lds(xs,xn,red);
  int c=blockIdx.x*64+(t&63), g=t>>6; float s=0;
  for(int i=g;i<D;i+=4) s+=xn[i]*bf2f(wq[(size_t)i*(HQ*DH)+c]);
  red[t]=s; __syncthreads();
  if(t<64) q[c]=red[t]+red[t+64]+red[t+128]+red[t+192];
}
__global__ void k_attn(const float* q, const u16* K, const u16* V, float* po){
  __shared__ float qs[DH], p[KPS], red[256];
  int h=blockIdx.x/NS, s=blockIdx.x%NS, t=threadIdx.x;
  if(t<DH) qs[t]=q[h*DH+t]; __syncthreads();
  const u16* kr=K+((size_t)h*SK+(SK-W)+s*KPS+t)*DH;
  float a=0; for(int d=0;d<DH;d++) a+=qs[d]*bf2f(kr[d]);
  a*=0.07216878364870322f;
  red[t]=a; __syncthreads();
  for(int o=128;o>0;o>>=1){ if(t<o) red[t]=fmaxf(red[t],red[t+o]); __syncthreads(); }
  float m=red[0]; __syncthreads();
  float e=__expf(a-m); p[t]=e; red[t]=e; __syncthreads();
  for(int o=128;o>0;o>>=1){ if(t<o) red[t]+=red[t+o]; __syncthreads(); }
  float l=red[0];
  float* out=po+(size_t)blockIdx.x*256;
  if(t<DH){ const u16* vr=V+((size_t)h*SK+(SK-W)+s*KPS)*DH+t; float o=0; for(int k=0;k<KPS;k++) o+=p[k]*bf2f(vr[(size_t)k*DH]); out[t]=o; }
  if(t==0){ out[192]=m; out[193]=l; }
}
__global__ void k_wo(const float* po, const u16* wo, float* part){
  __shared__ float o[DH];
  int h=blockIdx.x, t=threadIdx.x;
  if(t<DH){ float M=-INFINITY; for(int s=0;s<NS;s++) M=fmaxf(M,po[(h*NS+s)*256+192]);
    float L=0,a=0; for(int s=0;s<NS;s++){ float w=__expf(po[(h*NS+s)*256+192]-M); L+=w*po[(h*NS+s)*256+193]; a+=w*po[(h*NS+s)*256+t]; }
    o[t]=a/L; }
  __syncthreads();
  float s=0; for(int d=0;d<DH;d++) s+=o[d]*bf2f(wo[(size_t)(h*DH+d)*D+t]);
  part[h*D+t]=s;
}
__global__ void k_red(const float* xin, const float* part, int n, float* xout, __hip_bfloat16* out){
  int t=threadIdx.x; float s=0; for(int i=0;i<n;i++) s+=part[i*D+t];
  float r=xin[t]+s; xout[t]=r; if(out) out[t]=__float2bfloat16(r);
}
__global__ void k_mlp(const float* xf, const u16* w1, const u16* b1, const u16* w2, float* part){
  __shared__ float xs[D], xn[D], red[256], act[64];
  int t=threadIdx.x; for(int i=t;i<D;i+=256) xs[i]=xf[i]; __syncthreads();
  ln_to_lds(xs,xn,red);
  int c=blockIdx.x*64+(t&63), g=t>>6; float s=0;
  for(int i=g;i<D;i+=4) s+=xn[i]*bf2f(w1[(size_t)i*F+c]);
  red[t]=s; __syncthreads();
  if(t<64){ float h=red[t]+red[t+64]+red[t+128]+red[t+192]+bf2f(b1[c]);
    float sp=log1pf(expf(-fabsf(h)))+fmaxf(h,0.f); act[t]=h*tanhf(sp); }
  __syncthreads();
  for(int j=t;j<D;j+=256){ float a=0; for(int f=0;f<64;f++) a+=act[f]*bf2f(w2[(size_t)(blockIdx.x*64+f)*D+j]); part[blockIdx.x*D+j]=a; }
}
extern "C" void kernel_launch(void* const* d_in, const int* in_sizes, int n_in, void* d_out, int out_size, void* d_ws, size_t ws_size, hipStream_t stream){
  const u16* x=(const u16*)d_in[0]; const u16* K=(const u16*)d_in[1]; const u16* V=(const u16*)d_in[2];
  const u16* wq=(const u16*)d_in[3]; const u16* wo=(const u16*)d_in[4];
  const u16* w1=(const u16*)d_in[6]; const u16* b1=(const u16*)d_in[7]; const u16* w2=(const u16*)d_in[8];
  float* ws=(float*)d_ws; float* xa=ws; float* xb=ws+1024; float* q=ws+2048; float* po=ws+16384; float* part=ws+16384+65536;
  k_init<<<3,256,0,stream>>>(x,xa);
  for(int it=0;it<2;it++){
    k_q<<<96,256,0,stream>>>(xa,wq,q);
    k_attn<<<256,256,0,stream>>>(q,K,V,po);
    k_wo<<<32,640,0,stream>>>(po,wo,part);
    k_red<<<1,640,0,stream>>>(xa,part,32,xb,nullptr);
    k_mlp<<<24,256,0,stream>>>(xb,w1,b1,w2,part);
    k_red<<<1,640,0,stream>>>(xb,part,24,xa,it==1?(__hip_bfloat16*)d_out:nullptr);
  }
}
```

```cpp
#include <hip/hip_runtime.h>
typedef unsigned v4u __attribute__((ext_vector_type(4)));
typedef unsigned v2u __attribute__((ext_vector_type(2)));
typedef float v4f __attribute__((ext_vector_type(4)));
typedef float v2f __attribute__((ext_vector_type(2)));
typedef short v4s __attribute__((ext_vector_type(4)));
typedef __bf16 v8b __attribute__((ext_vector_type(8)));
typedef __bf16 v2b __attribute__((ext_vector_type(2)));
typedef unsigned long long u64;
typedef __amdgpu_buffer_rsrc_t rsrc_t;
#define LAS __attribute__((address_space(3)))
#define DEV __device__ __forceinline__

#define SL_O 5
#define SL_X 10
#define SL_MB 10
#define SL_MN 50
#define SL_Q 5
#define SL_RETRY 1
constexpr unsigned CAP = 40000;

constexpr int WS_FLAG = 1024, WS_BOX = 4096, BOX = 53 * 128;
constexpr int Q0 = 0, CS = 768, Q1 = 1536, ST0 = 2304, ST1 = 2432, O0 = 2560, O1 = 3072, X0 = 3584, X1 = 3840, M0 = 4096, M1 = 5376;
constexpr int WS_END = WS_BOX + 256 * BOX;
constexpr unsigned OOB = 0x7fffff00u;
constexpr int NT = 2, SC1 = 16;

constexpr int L_STG = 0;
constexpr int L_KS = 36864;
constexpr int L_XS = L_KS + 8 * 6400;
constexpr int L_XR = L_XS + 16 * 208;
constexpr int L_QL = L_XR + 320;
constexpr int L_OW = L_QL + 384;
constexpr int L_ML = L_OW + 6400;
constexpr int L_XP = L_ML + 64;
constexpr int L_HP = L_XP + 3072;
constexpr int L_S8 = L_HP + 512;
constexpr int L_HC = L_S8 + 64;
constexpr int L_END = L_HC + 512;

DEV rsrc_t mkrs(const void* p, unsigned bytes) { return __builtin_amdgcn_make_buffer_rsrc((void*)p, (short)0, (int)bytes, 0x00020000); }
template <int AUX> DEV v4u ld4(rsrc_t r, unsigned vo, unsigned so = 0) { return __builtin_amdgcn_raw_buffer_load_b128(r, vo, so, AUX); }
template <int AUX> DEV v2u ld2(rsrc_t r, unsigned vo, unsigned so = 0) { return __builtin_amdgcn_raw_buffer_load_b64(r, vo, so, AUX); }
template <int AUX> DEV unsigned ld1(rsrc_t r, unsigned vo, unsigned so = 0) { return __builtin_amdgcn_raw_buffer_load_b32(r, vo, so, AUX); }
DEV unsigned canon(unsigned v) { return min(v, 0xfffffffeu); }
DEV void st1(rsrc_t r, unsigned vo, unsigned so, unsigned v) { __builtin_amdgcn_raw_buffer_store_b32(canon(v), r, vo, so, SC1); }
DEV void st2(rsrc_t r, unsigned vo, unsigned so, v2u v) { v2u c = {canon(v.x), canon(v.y)}; __builtin_amdgcn_raw_buffer_store_b64(c, r, vo, so, SC1); }
DEV void fence() { asm volatile("" ::: "memory"); __builtin_amdgcn_sched_barrier(0); }
DEV void bar() { asm volatile("s_waitcnt lgkmcnt(0)" ::: "memory"); __builtin_amdgcn_s_barrier(); fence(); }
DEV v4f mfma(v4u a, v4u b, v4f c) { return __builtin_amdgcn_mfma_f32_16x16x32_bf16(__builtin_bit_cast(v8b, a), __builtin_bit_cast(v8b, b), c, 0, 0, 0); }
DEV v4f mfma16(v2u a, v2u b, v4f c) { return __builtin_amdgcn_mfma_f32_16x16x16bf16_1k(__builtin_bit_cast(v4s, a), __builtin_bit_cast(v4s, b), c, 0, 0, 0); }
DEV unsigned pk(float a, float b) { v2f v = {a, b}; return __builtin_bit_cast(unsigned, __builtin_convertvector(v, v2b)); }
DEV float lo16(unsigned v) { return __uint_as_float(v << 16); }
DEV float hi16(unsigned v) { return __uint_as_float(v & 0xffff0000u); }
template <int C> DEV float dpp(float x) { return __uint_as_float(__builtin_amdgcn_update_dpp(0u, __float_as_uint(x), C, 0xf, 0xf, true)); }
template <int C> DEV unsigned dppu(unsigned x) { return __builtin_amdgcn_update_dpp(0u, x, C, 0xf, 0xf, true); }
constexpr int X1P = 0xB1, X2P = 0x4E, HMIR = 0x141, ROR = 0x120, BCAST = 0x150;
DEV float sum8(float x) { x += dpp<X1P>(x); x += dpp<X2P>(x); x += dpp<HMIR>(x); return x; }
DEV float max8(float x) { x = fmaxf(x, dpp<X1P>(x)); x = fmaxf(x, dpp<X2P>(x)); x = fmaxf(x, dpp<HMIR>(x)); return x; }
DEV float sum16(float x) { x = sum8(x); x += dpp<ROR + 8>(x); return x; }
DEV float add16(float x) { auto s = __builtin_amdgcn_permlane16_swap(__float_as_uint(x), __float_as_uint(x), false, false); return __uint_as_float(s[0]) + __uint_as_float(s[1]); }
DEV float add32(float x) { auto s = __builtin_amdgcn_permlane32_swap(__float_as_uint(x), __float_as_uint(x), false, false); return __uint_as_float(s[0]) + __uint_as_float(s[1]); }
DEV float mx16(float x) { auto s = __builtin_amdgcn_permlane16_swap(__float_as_uint(x), __float_as_uint(x), false, false); return fmaxf(__uint_as_float(s[0]), __uint_as_float(s[1])); }
DEV float mx32(float x) { auto s = __builtin_amdgcn_permlane32_swap(__float_as_uint(x), __float_as_uint(x), false, false); return fmaxf(__uint_as_float(s[0]), __uint_as_float(s[1])); }
DEV unsigned mx4(unsigned m, v4u v) { return max(max(m, max(v.x, v.y)), max(v.z, v.w)); }
DEV bool bad(unsigned m) { return __builtin_amdgcn_ballot_w64(m == 0xffffffffu) != 0; }
template <class T> DEV T lrd(LAS char* l, int off) { return *(LAS T*)(l + off); }
template <class T> DEV void lwr(LAS char* l, int off, T v) { *(LAS T*)(l + off) = v; }
DEV v2u trd(LAS char* l, int off) { return __builtin_bit_cast(v2u, __builtin_amdgcn_ds_read_tr16_b64_v4i16((LAS v4s*)(l + off))); }
DEV v4u trd8(LAS char* l, int off, int rs) { v2u a = trd(l, off), b = trd(l, off + 4 * rs); return v4u{a.x, a.y, b.x, b.y}; }
DEV v2u hilo(v4f d, bool lo) {
  unsigned a = pk(d[0], d[1]), b = pk(d[2], d[3]);
  unsigned c = pk(d[0] - lo16(a), d[1] - hi16(a)), e = pk(d[2] - lo16(b), d[3] - hi16(b));
  return lo ? v2u{c, e} : v2u{a, b};
}
#define STAMP(n)
#define CNT(n, v)

__global__ void __launch_bounds__(512, 2)
k_fused(const void* x, const void* Kc, const void* Vc, const void* wq, const void* wo, const void* w1, const void* b1, const void* w2, unsigned short* out, char* ws) {
  __shared__ __attribute__((aligned(16))) char smem[L_END];
  LAS char* L = (LAS char*)smem;
  const int b = blockIdx.x, tid = threadIdx.x, lane = tid & 63, w = __builtin_amdgcn_readfirstlane(tid >> 6);
  const int xcd = b & 7, bi = b >> 3, r = xcd * 4 + (bi >> 3), s = bi & 7, id = xcd * 32 + bi;
  const bool isB = bi < 12, isF = (xcd == 0 && bi >= 24);
  const int pB = xcd * 12 + bi;
  const int g = lane >> 4, i = lane & 15, u = lane & 7, qd = lane >> 3, tq = i >> 2, tp = i & 3;
  STAMP(0);
  const rsrc_t rW = mkrs(ws, WS_END);
  const unsigned myBox = WS_BOX + id * BOX;
  v4u t4[4];
  {
    const rsrc_t rq = mkrs(wq, 640 * 6144 * 2);
#pragma unroll
    for (int j = 0; j < 4; ++j) { int ch = j * 512 + tid, row = ch / 24, cc = ch % 24;
      t4[j] = ld4<NT>(rq, ch < 1920 ? row * 12288 + cc * 16 : OOB, 80 * s * 12288 + r * 384); }
  }
  v2u xin = ld2<0>(mkrs(x, 1280), (w == 0 && g < 2 && i < 10) ? 16 * i + 8 * g : OOB, 160 * s);
  u64 epoch = 0;
  if (w == 7) {
    v2u e = ld2<SC1>(rW, 0);
    const v4u ones = {~0u, ~0u, ~0u, ~0u};
#pragma unroll
    for (int j = 0; j < 7; ++j) __builtin_amdgcn_raw_buffer_store_b128(ones, rW, (j * 1024 + lane * 16 < BOX) ? j * 1024 + lane * 16 : OOB, myBox, SC1);
    asm volatile("s_waitcnt vmcnt(0)" ::: "memory");
    epoch = (((u64)e.y << 32) | e.x) + 1;
    const v2u ev = {(unsigned)epoch, (unsigned)(epoch >> 32)};
    __builtin_amdgcn_raw_buffer_store_b64(ev, rW, lane == 0 ? id * 8 : OOB, WS_FLAG, SC1);
    __builtin_amdgcn_s_sleep(16);
    for (unsigned it = 0; it < CAP; ++it) {
      fence();
      v4u f0 = ld4<SC1>(rW, lane * 32, WS_FLAG), f1 = ld4<SC1>(rW, lane * 32 + 16, WS_FLAG);
      bool ok = f0.x == ev.x && f0.z == ev.x && f1.x == ev.x && f1.z == ev.x && f0.y == ev.y && f0.w == ev.y && f1.y == ev.y && f1.w == ev.y;
      if (__builtin_amdgcn_ballot_w64(!ok) == 0) break;
      __builtin_amdgcn_s_sleep(4);
    }
  }
  fence();
  STAMP(1);
  const unsigned kvBase = ((unsigned)r * 8192 + 6144 + s * 256 + w * 32) * 384;
  v4u kf[12];
  {
    const rsrc_t rk = mkrs(Kc, 32u * 8192 * 384);
#pragma unroll
    for (int j = 0; j < 12; ++j) kf[j] = ld4<NT>(rk, qd * 384 + u * 16 + (j / 3) * 3072 + (j % 3) * 128, kvBase);
  }
  fence();
#pragma unroll
  for (int j = 0; j < 4; ++j) lwr<v4u>(L, L_STG + (j * 512 + tid) * 16, t4[j]);
  if (i < 12) lwr<v2u>(L, L_XS + (w + 8 * (g >> 1)) * 208 + 16 * i + 8 * (g & 1), xin);
  bar();
  STAMP(2);
  v4u wqf[6];
  if (w < 6) {
#pragma unroll
    for (int n = 0; n < 2; ++n)
#pragma unroll
      for (int kb = 0; kb < 3; ++kb) {
        v4u f = trd8(L, L_STG + (32 * kb + 8 * g + tq) * 384 + 32 * (2 * w + n) + 8 * tp, 384);
        if (kb == 2 && g >= 2) f = v4u{0, 0, 0, 0};
        wqf[n * 3 + kb] = f;
      }
  } else {
    const unsigned o = i == 0 ? 0x3f803f80u : 0u;
#pragma unroll
    for (int kb = 0; kb < 6; ++kb) wqf[kb] = v4u{o, o, o, o};
  }
  const v4f z4 = {0.f, 0.f, 0.f, 0.f};
  auto rowsum2 = [&](v4f d0, v4f d1) {
    float a = (d0[0] + d0[1]) + (d0[2] + d0[3]), c = (d1[0] + d1[1]) + (d1[2] + d1[3]);
    auto sw = __builtin_amdgcn_permlane32_swap(__float_as_uint(a), __float_as_uint(c), false, false);
    return add16(__uint_as_float(sw[0]) + __uint_as_float(sw[1]));
  };
  auto phaseQ = [&](int QO, int SO) {
    if (w < 7) {
      v4u a[3];
#pragma unroll
      for (int kb = 0; kb < 3; ++kb) a[kb] = lrd<v4u>(L, L_XS + i * 208 + 64 * kb + 16 * g);
      v4f d0 = z4, d1 = z4;
      if (w < 6) {
#pragma unroll
        for (int kb = 0; kb < 3; ++kb) { d0 = mfma(a[kb], wqf[kb], d0); d1 = mfma(a[kb], wqf[3 + kb], d1); }
        float v = rowsum2(d0, d1);
        st1(rW, (g & 1) ? OOB : 128 * w + 32 * g + 4 * i, myBox + QO, __float_as_uint(v));
      } else {
#pragma unroll
        for (int kb = 0; kb < 3; ++kb) { d0 = mfma(a[kb], a[kb], d0); d1 = mfma(a[kb], wqf[3 + kb], d1); }
        float v = sum16(rowsum2(d0, d1));
        st1(rW, (lane & 31) ? OOB : 2 * g, myBox + SO, __float_as_uint(v));
      }
    } else if (lane < 40) {
      float a = 0.f, c = 0.f;
#pragma unroll
      for (int k = 0; k < 16; ++k) { unsigned v = lrd<unsigned>(L, L_XS + k * 208 + 4 * lane); a += lo16(v); c += hi16(v); }
      lwr<v2f>(L, L_XR + 8 * lane, v2f{a, c});
    }
  };
  phaseQ(Q0, ST0);
  if (w < 6) {
    const unsigned o = i == 0 ? 0x3f803f80u : 0u;
    const v4u a = {o, o, o, o};
    v4f d0 = z4, d1 = z4;
#pragma unroll
    for (int kb = 0; kb < 3; ++kb) { d0 = mfma(a, wqf[kb], d0); d1 = mfma(a, wqf[3 + kb], d1); }
    float v = rowsum2(d0, d1);
    st1(rW, (g & 1) ? OOB : 128 * w + 32 * g + 4 * i, myBox + CS, __float_as_uint(v));
  }
  fence();
  STAMP(3);
  v4u va[8]; v2u vb[8];
  const rsrc_t rv = mkrs(Vc, 32u * 8192 * 384);
#pragma unroll
  for (int j = 0; j < 4; ++j) { va[j] = ld4<NT>(rv, g * 1536 + i * 16 + j * 384, kvBase); vb[j] = ld2<NT>(rv, g * 1536 + i * 8 + 256 + j * 384, kvBase); }
  fence();
  const unsigned rowBox = WS_BOX + r * 8 * BOX;
  const unsigned qoff = qd < 6 ? u * BOX + 96 * w + 16 * qd : OOB;
  v4u pq = ld4<SC1>(rW, qoff, rowBox + Q0), pc = ld4<SC1>(rW, qoff, rowBox + CS);
  v2u ps = ld2<SC1>(rW, u * BOX, rowBox + ST0);
  fence();
#pragma unroll
  for (int j = 4; j < 8; ++j) { va[j] = ld4<NT>(rv, g * 1536 + i * 16 + (j - 4) * 384, kvBase + 6144); vb[j] = ld2<NT>(rv, g * 1536 + i * 8 + 256 + (j - 4) * 384, kvBase + 6144); }
  fence();
  STAMP(4);
#pragma unroll
  for (int h = 0; h < 2; ++h) {
#pragma unroll
    for (int j = 0; j < 6; ++j) lwr<v4u>(L, L_KS + w * 6400 + (8 * (j / 3) + qd) * 400 + (j % 3) * 128 + u * 16, kf[6 * h + j]);
    asm volatile("" ::: "memory");
#pragma unroll
    for (int j = 0; j < 6; ++j) kf[6 * h + j] = lrd<v4u>(L, L_KS + w * 6400 + i * 400 + 64 * j + 16 * g);
    asm volatile("" ::: "memory");
  }
  STAMP(5);
  {
    unsigned it = 0;
    while (bad(max(mx4(mx4(0, pq), pc), max(ps.x, ps.y))) && ++it < CAP) {
      __builtin_amdgcn_s_sleep(SL_RETRY); fence();
      pq = ld4<SC1>(rW, qoff, rowBox + Q0); pc = ld4<SC1>(rW, qoff, rowBox + CS); ps = ld2<SC1>(rW, u * BOX, rowBox + ST0);
    }
    CNT(24, it);
  }
  v4f cs;
#pragma unroll
  for (int k = 0; k < 4; ++k) cs[k] = sum8(__uint_as_float(pc[k]));
  auto qsum = [&](v4u pq, v2u ps) {
    float sxx = sum8(__uint_as_float(ps.x)), sx = sum8(__uint_as_float(ps.y));
    float mean = sx * (1.f / 640), var = fmaxf(sxx * (1.f / 640) - mean * mean, 0.f);
    float rs = __builtin_amdgcn_rsqf(var + 1e-5f) * (0.07216878364870322f * 1.4426950408889634f);
    float q[4];
#pragma unroll
    for (int k = 0; k < 4; ++k) q[k] = (sum8(__uint_as_float(pq[k])) - mean * cs[k]) * rs;
    if (u == 0 && qd < 6) lwr<v2u>(L, L_QL + 48 * w + 8 * qd, v2u{pk(q[0], q[1]), pk(q[2], q[3])});
    bar();
  };
  float mw, lw; v4u pa;
  auto qk = [&]() {
    v4f s0 = z4, s1 = z4;
#pragma unroll
    for (int kb = 0; kb < 6; ++kb) { v4u q = lrd<v4u>(L, L_QL + 64 * kb + 16 * g); s0 = mfma(kf[kb], q, s0); s1 = mfma(kf[6 + kb], q, s1); }
    float m = fmaxf(fmaxf(fmaxf(s0[0], s0[1]), fmaxf(s0[2], s0[3])), fmaxf(fmaxf(s1[0], s1[1]), fmaxf(s1[2], s1[3])));
    m = mx32(mx16(m));
#pragma unroll
    for (int k = 0; k < 4; ++k) { s0[k] = __builtin_amdgcn_exp2f(s0[k] - m); s1[k] = __builtin_amdgcn_exp2f(s1[k] - m); }
    pa = v4u{pk(s0[0], s0[1]), pk(s0[2], s0[3]), pk(s1[0], s1[1]), pk(s1[2], s1[3])};
    mw = m;
    lw = add32(add16(((s0[0] + s0[1]) + (s0[2] + s0[3])) + ((s1[0] + s1[1]) + (s1[2] + s1[3]))));
  };
  qsum(pq, ps);
  qk();
  fence();
  STAMP(6);
  v4u t1[3], t2[3]; unsigned b1v = 0;
  {
    const rsrc_t ro = mkrs(wo, 6144 * 640 * 2);
#pragma unroll
    for (int j = 0; j < 4; ++j) { int ch = j * 512 + tid, row = ch / 10, cc = ch % 10;
      t4[j] = ld4<NT>(ro, ch < 1920 ? row * 1280 + cc * 16 : OOB, 192 * r * 1280 + s * 160); }
    if (isB) {
      const rsrc_t r1 = mkrs(w1, 640 * 1536 * 2), r2 = mkrs(w2, 1536 * 640 * 2);
#pragma unroll
      for (int j = 0; j < 3; ++j) { int ch = j * 64 + lane;
        t1[j] = ld4<0>(r1, ch < 160 ? (ch >> 1) * 3072 + (ch & 1) * 16 : OOB, 80 * w * 3072 + pB * 32);
        t2[j] = ld4<NT>(r2, ch < 160 ? (ch / 10) * 1280 + (ch % 10) * 16 : OOB, 16 * pB * 1280 + w * 160); }
      b1v = __builtin_amdgcn_raw_buffer_load_b16(mkrs(b1, 3072), 2 * (4 * g + tp), 32 * pB, 0);
    }
  }
  fence();
  STAMP(7);
  v4u vf[12];
#pragma unroll
  for (int k = 0; k < 4; ++k) {
#pragma unroll
    for (int c = 0; c < 4; ++c) {
      vf[2 * c][k] = __builtin_amdgcn_perm(va[2 * k + 1][c], va[2 * k][c], 0x05040100u);
      vf[2 * c + 1][k] = __builtin_amdgcn_perm(va[2 * k + 1][c], va[2 * k][c], 0x07060302u);
    }
#pragma unroll
    for (int c = 0; c < 2; ++c) {
      vf[8 + 2 * c][k] = __builtin_amdgcn_perm(vb[2 * k + 1][c], vb[2 * k][c], 0x05040100u);
      vf[9 + 2 * c][k] = __builtin_amdgcn_perm(vb[2 * k + 1][c], vb[2 * k][c], 0x07060302u);
    }
  }
  auto pv = [&](int OO) {
    float o[12];
#pragma unroll
    for (int t = 0; t < 12; ++t) o[t] = mfma(pa, vf[t], z4)[0];
    if (g == 0) {
      lwr<v4f>(L, L_OW + w * 800 + 32 * i, v4f{o[0], o[1], o[2], o[3]});
      lwr<v4f>(L, L_OW + w * 800 + 32 * i + 16, v4f{o[4], o[5], o[6], o[7]});
      lwr<v4f>(L, L_OW + w * 800 + 512 + 16 * i, v4f{o[8], o[9], o[10], o[11]});
      if (i == 0) lwr<v2f>(L, L_ML + 8 * w, v2f{mw, lw});
    }
    bar();
    v2f ml = lrd<v2f>(L, L_ML + 8 * u);
    v4f ov = lrd<v4f>(L, L_OW + u * 800 + 96 * w + 16 * qd);
    float M = max8(ml.x), e = __builtin_amdgcn_exp2f(ml.x - M), Lsum = sum8(e * ml.y);
    float f = e * __builtin_amdgcn_rcpf(Lsum);
#pragma unroll
    for (int k = 0; k < 4; ++k) ov[k] = sum8(ov[k] * f);
    unsigned lse = __float_as_uint(M + __builtin_amdgcn_logf(Lsum));
    v2u val = qd < 6 ? v2u{pk(ov[0], ov[1]), pk(ov[2], ov[3])} : v2u{lse, lse};
    st2(rW, u != 0 ? OOB : qd < 6 ? 48 * w + 8 * qd : (qd == 6 && w == 0) ? 384 : OOB, myBox + OO, val);
    fence();
  };
  pv(O0);
  STAMP(8);
#pragma unroll
  for (int j = 0; j < 4; ++j) lwr<v4u>(L, L_STG + (j * 512 + tid) * 16, t4[j]);
  bar();
  v4u wof[6];
#pragma unroll
  for (int kb = 0; kb < 6; ++kb) wof[kb] = trd8(L, L_STG + (32 * kb + 8 * g + tq) * 160 + 32 * (w < 5 ? w : 0) + 8 * tp, 160);
  fence();
  STAMP(9);
  auto phaseO = [&](int OO, int XO, int sl, int cn) {
    if (w < 5) {
      float res = lrd<float>(L, L_XR + 64 * w + 4 * i) * (1.f / 32);
      if (sl) __builtin_amdgcn_s_sleep(SL_O);
      v4u a[6]; unsigned ls; unsigned it = 0;
      for (;;) {
        fence();
        ls = ld1<SC1>(rW, ((4 * g + i) & 7) * BOX + 384, rowBox + OO);
#pragma unroll
        for (int kb = 0; kb < 6; ++kb) a[kb] = ld4<SC1>(rW, u * BOX + 16 * g + 64 * kb, rowBox + OO);
        unsigned m = ls;
#pragma unroll
        for (int kb = 0; kb < 6; ++kb) m = mx4(m, a[kb]);
        if (!bad(m) || ++it >= CAP) break;
        __builtin_amdgcn_s_sleep(SL_RETRY);
      }
      CNT(cn, it);
      v4f d = z4;
#pragma unroll
      for (int kb = 0; kb < 6; ++kb) d = mfma(a[kb], wof[kb], d);
      float l = __uint_as_float(ls), e = __builtin_amdgcn_exp2f(l - max8(l));
      float f = e * __builtin_amdgcn_rcpf(sum8(e));
      float v = (dpp<BCAST + 0>(f) * d[0] + dpp<BCAST + 1>(f) * d[1]) + (dpp<BCAST + 2>(f) * d[2] + dpp<BCAST + 3>(f) * d[3]);
      v = add16(v) + res;
      st1(rW, (g == 0 && !(i & 1)) ? 32 * w + 2 * i : OOB, myBox + XO, pk(v, dpp<X1P>(v)));
    }
    fence();
  };
  phaseO(O0, X0, 0, 25);
  STAMP(10);
  v4u w1f[3]; v2u w2f[5]; float cs1 = 0.f, bias = 0.f;
  v4u del = {0, 0, 0, 0};
#pragma unroll
  for (int k = 0; k < 4; ++k) del[k] = ((u >> 1) == k) ? (0x3f80u << (16 * (i & 1))) : 0u;
  if (isB) {
    const int base = L_KS + w * 6400;
#pragma unroll
    for (int j = 0; j < 3; ++j) { lwr<v4u>(L, base + (j * 64 + lane) * 16, t1[j]); lwr<v4u>(L, base + 3072 + (j * 64 + lane) * 16, t2[j]); }
    asm volatile("" ::: "memory");
#pragma unroll
    for (int kb = 0; kb < 3; ++kb) w1f[kb] = trd8(L, base + (32 * kb + 8 * g + tq) * 32 + 8 * tp, 32);
#pragma unroll
    for (int t = 0; t < 5; ++t) w2f[t] = trd(L, base + 3072 + (4 * g + tq) * 160 + 32 * t + 8 * tp);
    const v4u on = {0x3f803f80u, 0x3f803f80u, 0x3f803f80u, 0x3f803f80u};
    v4f d = z4;
#pragma unroll
    for (int kb = 0; kb < 3; ++kb) d = mfma(on, w1f[kb], d);
    if (g == 0) lwr<float>(L, L_HC + 32 * i + 4 * w, d[0]);
    bias = lo16(b1v);
  }
  bar();
  if (isB) { v2f c = lrd<v2f>(L, L_HC + 32 * (4 * g + tp) + 8 * tq); cs1 = c.x + c.y; cs1 += dpp<ROR + 8>(cs1); cs1 += dpp<ROR + 4>(cs1); }
  auto phaseB = [&](int XO, int MO, int cn) {
    __builtin_amdgcn_s_sleep(SL_X);
    v4u a[8]; unsigned it = 0;
    for (;;) {
      fence();
#pragma unroll
      for (int q = 0; q < 8; ++q) a[q] = ld4<SC1>(rW, i < 10 ? g * 8 * BOX + 16 * i : OOB, WS_BOX + (32 * q + w) * BOX + XO);
      unsigned m = 0;
#pragma unroll
      for (int q = 0; q < 8; ++q) m = mx4(m, a[q]);
      if (!bad(m) || ++it >= CAP) break;
      __builtin_amdgcn_s_sleep(SL_RETRY);
    }
    CNT(cn, it);
    v4f d = z4;
#pragma unroll
    for (int q = 0; q < 8; ++q) d = mfma(del, a[q], d);
    const int xp = L_XP + w * 384;
    if (i < 12) lwr<v2u>(L, xp + (g >> 1) * 192 + 16 * i + 8 * (g & 1), hilo(d, g >= 2));
    asm volatile("" ::: "memory");
    v4u xa[3];
#pragma unroll
    for (int kb = 0; kb < 3; ++kb) xa[kb] = lrd<v4u>(L, xp + (i & 1) * 192 + 64 * kb + 16 * g);
    float ra = lo16(lrd<unsigned short>(L, xp + 2 * lane)) + lo16(lrd<unsigned short>(L, xp + 192 + 2 * lane));
    float rb = lo16(lrd<unsigned short>(L, xp + 128 + 2 * i)) + lo16(lrd<unsigned short>(L, xp + 192 + 128 + 2 * i));
    float sx = (d[0] + d[1]) + (d[2] + d[3]), sxx = (d[0] * d[0] + d[1] * d[1]) + (d[2] * d[2] + d[3] * d[3]);
    float sv = add16(sum16(g >= 2 ? sx : sxx));
    v4f h = z4;
#pragma unroll
    for (int kb = 0; kb < 3; ++kb) h = mfma(xa[kb], w1f[kb], h);
    if ((lane & 31) == 0) lwr<float>(L, L_S8 + 8 * w + 2 * g, sv);
    if (g == 0) lwr<float>(L, L_HP + 32 * i + 4 * w, h[0] + h[1]);
    bar();
    v2f hp = lrd<v2f>(L, L_HP + 32 * (4 * g + tp) + 8 * tq);
    v4f sp = lrd<v4f>(L, L_S8 + 16 * tq);
    float hr = hp.x + hp.y, txx = sp[0] + sp[2], tx = sp[1] + sp[3];
    hr += dpp<ROR + 8>(hr); txx += dpp<ROR + 8>(txx); tx += dpp<ROR + 8>(tx);
    hr += dpp<ROR + 4>(hr); txx += dpp<ROR + 4>(txx); tx += dpp<ROR + 4>(tx);
    float mean = tx * (1.f / 640), var = fmaxf(txx * (1.f / 640) - mean * mean, 0.f);
    float hh = (hr - mean * cs1) * __builtin_amdgcn_rsqf(var + 1e-5f) + bias;
    float e = __builtin_amdgcn_exp2f(-fabsf(hh) * 1.4426950408889634f);
    float num = hh >= 0.f ? 1.f + 2.f * e : e * (e + 2.f), den = num + (hh >= 0.f ? 2.f * e * e : 2.f);
    float act = hh * num * __builtin_amdgcn_rcpf(den);
    unsigned pp = pk(act, dpp<X1P>(act));
    v2u af = {dppu<0x00>(pp), dppu<0xAA>(pp)};
    float y[5];
#pragma unroll
    for (int t = 0; t < 5; ++t) y[t] = mfma16(af, w2f[t], z4)[0];
    float ya = (g == 0 ? y[0] : g == 1 ? y[1] : g == 2 ? y[2] : y[3]) + ra * (1.f / 96), yb = y[4] + rb * (1.f / 96);
    float na = dpp<X1P>(ya), nb = dpp<X1P>(yb);
    unsigned v = (i & 1) ? pk(nb, yb) : pk(ya, na);
    st1(rW, !(i & 1) ? 2 * lane : g == 0 ? 126 + 2 * i : OOB, myBox + MO + 160 * w, v);
    fence();
  };
  auto gatherM = [&](int MO, int cn) {
    v4u a[3]; unsigned it = 0;
    for (;;) {
      fence();
#pragma unroll
      for (int q = 0; q < 3; ++q) a[q] = ld4<SC1>(rW, i < 10 ? g * BOX + 16 * i : OOB, WS_BOX + (32 * w + 4 * q) * BOX + MO + 160 * s);
      if (!bad(mx4(mx4(mx4(0, a[0]), a[1]), a[2])) || ++it >= CAP) break;
      __builtin_amdgcn_s_sleep(SL_RETRY);
    }
    CNT(cn, it);
    v4f d = z4;
#pragma unroll
    for (int q = 0; q < 3; ++q) d = mfma(del, a[q], d);
    if (i < 12) lwr<v2u>(L, L_XS + (w + 8 * (g >> 1)) * 208 + 16 * i + 8 * (g & 1), hilo(d, g >= 2));
    bar();
  };
  STAMP(11);
  if (isB) { phaseB(X0, M0, 26); STAMP(12); __builtin_amdgcn_s_sleep(SL_MB); } else __builtin_amdgcn_s_sleep(SL_MN);
  gatherM(M0, 27);
  STAMP(13);
  phaseQ(Q1, ST1);
  fence();
  STAMP(14);
  __builtin_amdgcn_s_sleep(SL_Q);
  {
    unsigned it = 0;
    for (;;) {
      fence();
      pq = ld4<SC1>(rW, qoff, rowBox + Q1); ps = ld2<SC1>(rW, u * BOX, rowBox + ST1);
      if (!bad(max(mx4(0, pq), max(ps.x, ps.y))) || ++it >= CAP) break;
      __builtin_amdgcn_s_sleep(SL_RETRY);
    }
    CNT(28, it);
  }
  STAMP(15);
  qsum(pq, ps);
  qk();
  pv(O1);
  STAMP(16);
  phaseO(O1, X1, 1, 29);
  STAMP(17);
  if (isB) { bar(); phaseB(X1, M1, 30); STAMP(18); }
  if (isF) {
    bar();
    __builtin_amdgcn_s_sleep(SL_MN);
    gatherM(M1, 31);
    if (w == 7) {
      if (lane < 40) {
        float a = 0.f, c = 0.f;
#pragma unroll
        for (int k = 0; k < 16; ++k) { unsigned v = lrd<unsigned>(L, L_XS + k * 208 + 4 * lane); a += lo16(v); c += hi16(v); }
        ((unsigned*)out)[40 * s + lane] = pk(a, c);
      }
      if (s == 0 && lane == 0) __builtin_amdgcn_raw_buffer_store_b64(v2u{(unsigned)epoch, (unsigned)(epoch >> 32)}, rW, 0, 0, SC1);
    }
    STAMP(19);
  }
}

extern "C" void kernel_launch(void* const* d_in, const int* in_sizes, int n_in, void* d_out, int out_size, void* d_ws, size_t ws_size, hipStream_t stream) {
  k_fused<<<256, 512, 0, stream>>>(d_in[0], d_in[1], d_in[2], d_in[3], d_in[4], d_in[6], d_in[7], d_in[8], (unsigned short*)d_out, (char*)d_ws);
}
```

```cpp
#include <hip/hip_runtime.h>
typedef unsigned v8u __attribute__((ext_vector_type(8)));
typedef unsigned v4u __attribute__((ext_vector_type(4)));
typedef unsigned v2u __attribute__((ext_vector_type(2)));
typedef float v4f __attribute__((ext_vector_type(4)));
typedef float v2f __attribute__((ext_vector_type(2)));
typedef short v4s __attribute__((ext_vector_type(4)));
typedef __bf16 v16b __attribute__((ext_vector_type(16)));
typedef __bf16 v8b __attribute__((ext_vector_type(8)));
typedef __bf16 v2b __attribute__((ext_vector_type(2)));
typedef unsigned long long u64;
typedef __amdgpu_buffer_rsrc_t rsrc_t;
#define LAS __attribute__((address_space(3)))
#define DEV __device__ __forceinline__

#define SL_O0 2
#define SL_O 5
#define SL_X0 20
#define SL_X1 40
#define SL_M 0
#define SL_I 10
#define SL_Q 5
#define SL_QB 40
#define SL_RETRY 1
constexpr int CAP = 40000;

constexpr int WS_FLAG = 1024, WS_BOX = 4096, BOX = 49 * 128;
constexpr int Q1 = 0, IND0 = 0, IND1 = 128, O0 = 512, O1 = 1024, XR2 = 1536, D0 = 2048, D1 = 3328, CLR = 4608;
constexpr int Q0 = CLR;
constexpr int WS_END = WS_BOX + 256 * BOX;
constexpr unsigned OOB = 0x7fff0000u;
constexpr int NT = 2, SC1 = 16;

constexpr int L_QI = 0;
constexpr int L_OI = L_QI + 8 * 448;
constexpr int L_OW = L_OI + 8 * 448;
constexpr int L_XS = L_OW + 8 * 832;
constexpr int L_HP = L_XS + 8 * 320;
constexpr int L_S8 = L_HP + 512;
constexpr int L_KS = L_S8 + 64;
constexpr int L_WQ = L_KS + 192 * 288;
constexpr int L_END = L_WQ + 128 * 416;

DEV rsrc_t mkrs(const void* p, unsigned bytes) { return __builtin_amdgcn_make_buffer_rsrc((void*)p, (short)0, (int)bytes, 0x00020000); }
template <int AUX> DEV v4u ld4(rsrc_t r, unsigned vo, unsigned so = 0) { return __builtin_amdgcn_raw_buffer_load_b128(r, vo, so, AUX); }
template <int AUX> DEV v2u ld2(rsrc_t r, unsigned vo, unsigned so = 0) { return __builtin_amdgcn_raw_buffer_load_b64(r, vo, so, AUX); }
template <int AUX> DEV unsigned ld1(rsrc_t r, unsigned vo, unsigned so = 0) { return __builtin_amdgcn_raw_buffer_load_b32(r, vo, so, AUX); }
DEV unsigned canon(unsigned v) { return min(v, 0xfffffffeu); }
DEV void st2r(rsrc_t r, unsigned vo, unsigned so, v2u v) { __builtin_amdgcn_raw_buffer_store_b64(v, r, vo, so, SC1); }
DEV void st4r(rsrc_t r, unsigned vo, unsigned so, v4u v) { __builtin_amdgcn_raw_buffer_store_b128(v, r, vo, so, SC1); }
DEV void SB() { __builtin_amdgcn_sched_barrier(0); }
DEV void fence() { asm volatile("" ::: "memory"); SB(); }
DEV void bar() { asm volatile("s_waitcnt lgkmcnt(0)" ::: "memory"); __builtin_amdgcn_s_barrier(); fence(); }
template <class T> DEV void pin(T& v) { asm volatile("" : "+v"(v)); }
DEV v4f zero4() { v4f z = {0.f, 0.f, 0.f, 0.f}; pin(z); return z; }
template <int AB> DEV v4f smf(v4u a, v8u b, v4f c, unsigned idx) { return __builtin_amdgcn_smfmac_f32_16x16x64_bf16(__builtin_bit_cast(v8b, a), __builtin_bit_cast(v16b, b), c, idx, 0, AB); }
DEV v8u cat(v4u a, v4u b) { return __builtin_shufflevector(a, b, 0, 1, 2, 3, 4, 5, 6, 7); }
DEV unsigned pk(float a, float b) { v2f v = {a, b}; return __builtin_bit_cast(unsigned, __builtin_convertvector(v, v2b)); }
DEV float lo16(unsigned v) { return __uint_as_float(v << 16); }
DEV float hi16(unsigned v) { return __uint_as_float(v & 0xffff0000u); }
template <int C> DEV float dpp(float x) { return __uint_as_float(__builtin_amdgcn_update_dpp(0u, __float_as_uint(x), C, 0xf, 0xf, true)); }
template <int C> DEV unsigned dppu(unsigned x) { return __builtin_amdgcn_update_dpp(0u, x, C, 0xf, 0xf, true); }
constexpr int X1P = 0xB1, X2P = 0x4E, HMIR = 0x141, SHL = 0x100, ROR = 0x120, BCAST = 0x150;
DEV float sum8(float x) { x += dpp<X1P>(x); x += dpp<X2P>(x); x += dpp<HMIR>(x); return x; }
DEV float sum16(float x) { x = sum8(x); x += dpp<ROR + 8>(x); return x; }
template <int S> DEV v4u col4(unsigned p) { return v4u{p, dppu<SHL + S>(p), dppu<SHL + 2 * S>(p), dppu<SHL + 3 * S>(p)}; }
#define DPPM " row_mask:0xf bank_mask:0xf\n"
DEV float max8(float x) { float m;
  asm("s_nop 1\n v_max_f32_dpp %0, %1, %1 quad_perm:[1,0,3,2]" DPPM "s_nop 1\n v_max_f32_dpp %0, %0, %0 quad_perm:[2,3,0,1]" DPPM
      "s_nop 1\n v_max_f32_dpp %0, %0, %0 row_half_mirror" DPPM : "=&v"(m) : "v"(x)); return m; }
DEV float max32(float& x) { float m, t;
  asm("s_nop 1\n v_max_f32_dpp %0, %2, %2 quad_perm:[1,0,3,2]" DPPM "v_mov_b32_dpp %2, %2 row_ror:8 row_mask:0xa bank_mask:0xf\n s_nop 0\n v_max_f32_dpp %0, %0, %0 quad_perm:[2,3,0,1]" DPPM
      "s_nop 1\n v_max_f32_dpp %0, %0, %0 row_half_mirror" DPPM "s_nop 1\n v_max_f32_dpp %1, %0, %0 row_mirror" DPPM "v_max_f32_dpp %0, %0, %0 row_mirror" DPPM
      "s_nop 1\n v_permlane32_swap_b32 %0, %1\n s_nop 0\n v_max_f32 %0, %0, %1" : "=&v"(m), "=&v"(t), "+v"(x)); return m; }
DEV float add16(float x) { auto s = __builtin_amdgcn_permlane16_swap(__float_as_uint(x), __float_as_uint(x), false, false); return __uint_as_float(s[0]) + __uint_as_float(s[1]); }
DEV float add32(float x) { auto s = __builtin_amdgcn_permlane32_swap(__float_as_uint(x), __float_as_uint(x), false, false); return __uint_as_float(s[0]) + __uint_as_float(s[1]); }
DEV float fold(v4f d) { v2f p = v2f{d[0], d[1]} + v2f{d[2], d[3]}; return p.x + p.y; }
DEV float rowsum2(float a, float c) {
  auto sw = __builtin_amdgcn_permlane32_swap(__float_as_uint(a), __float_as_uint(c), false, false);
  return add16(__uint_as_float(sw[0]) + __uint_as_float(sw[1]));
}
DEV unsigned mx4(v4u v) { return max(max(v.x, v.y), max(v.z, v.w)); }
DEV unsigned sel(bool c, unsigned a, unsigned b = OOB) { return c ? a : b; }
DEV bool anyl(bool c) { return __builtin_amdgcn_ballot_w64(c) != 0; }
template <class T> DEV T lrd(unsigned a) { return *(LAS T*)(a); }
template <class T> DEV void lwr(unsigned a, T v) { *(LAS T*)(a) = v; }
DEV v2u trd(unsigned a) { return __builtin_bit_cast(v2u, __builtin_amdgcn_ds_read_tr16_b64_v4i16((LAS v4s*)(a))); }
DEV v8u trd16(unsigned a, int rs) { v2u p = trd(a), q = trd(a + 4 * rs), r = trd(a + 32 * rs), s = trd(a + 36 * rs); return v8u{p.x, p.y, q.x, q.y, r.x, r.y, s.x, s.y}; }
DEV int kpair(int d) { return (d & ~7) | ((d & 3) << 1) | ((d >> 2) & 1); }
#define DUMP()
#define STAMP(n)
#define CNT(n, v)

__global__ void __launch_bounds__(512, 2)
k_fused(const void* x, const void* Kc, const void* Vc, const void* wq, const void* wo, const void* w1, const void* b1, const void* w2, void* out, char* ws) {
  __shared__ __attribute__((aligned(16))) char smem[L_END];
  const unsigned L = (unsigned)(size_t)(LAS char*)smem;
  const int b = blockIdx.x, tid = threadIdx.x, lane = tid & 63, w = __builtin_amdgcn_readfirstlane(tid >> 6);
  const int xcd = b & 7, bi = b >> 3, r = xcd * 4 + (bi >> 3), s = bi & 7;
  const bool isB = s >= 5, isF = r == 3;
  const int pB = 3 * r + s - 5;
  const int g = lane >> 4, i = lane & 15, u = lane & 7, qd = lane >> 3, tq = i >> 2, tp = i & 3;
  STAMP(0);
  const rsrc_t rW = mkrs(ws, WS_END);
  const unsigned rowBox = WS_BOX + r * 8 * BOX, myBox = rowBox + s * BOX, prtBox = rowBox + w * BOX;
  const u64 epoch = *(const __attribute__((address_space(4))) u64*)ws + 1;
  const unsigned tagL = (unsigned)epoch, tagH = (unsigned)(epoch >> 32);
  st4r(rW, lane < CLR / 128 ? lane * 16 : OOB, myBox + w * (CLR / 8), v4u{~0u, ~0u, ~0u, ~0u});
  fence();
  v4u t[6];
  unsigned b1v = 0;
  const rsrc_t r1 = mkrs(w1, 640 * 1536 * 2);
  const unsigned o1v = w < 5 ? (lane >> 1) * 3072 + (lane & 1) * 16 : OOB, o1s = 128 * w * 3072 + pB * 32;
  const unsigned a1w = L + L_WQ + w * 4096 + (2 * kpair(lane >> 2) + ((lane >> 1) & 1)) * 32 + 16 * (lane & 1);
  if (!isB) {
    const rsrc_t rq = mkrs(wq, 640 * 6144 * 2);
#pragma unroll
    for (int j = 0; j < 6; ++j) { int ch = j * 512 + tid; t[j] = ld4<NT>(rq, (ch / 24) * 12288 + (ch % 24) * 16, 128 * s * 12288 + r * 384); }
  } else {
#pragma unroll
    for (int j = 0; j < 2; ++j) t[j] = ld4<0>(r1, o1v, o1s + 32 * j * 3072);
    b1v = __builtin_amdgcn_raw_buffer_load_b16(mkrs(b1, 3072), 2 * (8 * (g & 1) + u), 32 * pB, 0);
  }
  v2u xin = ld2<0>(mkrs(x, 1280), (isB || w == 0) ? 16 * i + 8 * (g & 1) : OOB, 256 * (isB ? w : s));
  fence();
  __builtin_amdgcn_s_barrier();
  fence();
  const unsigned kvBase = ((unsigned)r * 8192 + 6144 + s * 256 + w * 32) * 384;
  const rsrc_t rk = mkrs(Kc, 32u * 8192 * 384);
  v4u kr[12];
#pragma unroll
  for (int j = 0; j < 6; ++j) kr[j] = ld4<NT>(rk, qd * 384 + u * 16 + (j / 3) * 3072 + (j % 3) * 128, kvBase);
  fence();
  STAMP(1);
  asm volatile("s_waitcnt vmcnt(6)" ::: "memory");
  unsigned aXSw = L + L_XS + w * (isB ? 256 : 320) + 16 * i + 8 * (g & 1); pin(aXSw);
  if (!isB) {
#pragma unroll
    for (int j = 0; j < 6; ++j) { int ch = j * 512 + tid, row = ch / 24; lwr<v4u>(L + L_WQ + (2 * kpair(row >> 1) + (row & 1)) * 416 + (ch % 24) * 16, t[j]); }
    lwr<v2u>(aXSw, xin);
  } else {
#pragma unroll
    for (int j = 0; j < 2; ++j) lwr<v4u>(a1w + 1024 * j, t[j]);
  }
  bar();
  STAMP(2);
  const unsigned idx = ((i & 1) ? 0xEEEEu : 0x4444u) | ((i & 2) ? 0xEEEE0000u : 0x44440000u);
  v4u del;
  { const unsigned o = (i & 1) ? 0x3f800000u : 0x3f80u; del = (i & 4) ? v4u{0, o, 0, o} : v4u{o, 0, o, 0}; }
  v8u wqf[4];
  auto readWq = [&]() {
    const unsigned a = L + L_WQ + 64 * (w < 6 ? w : 0) + 8 * tp + (8 * g + tq) * 416;
#pragma unroll
    for (int n = 0; n < 4; ++n) wqf[n] = trd16(a + 32 * (n >> 1) + 64 * 416 * (n & 1), 416);
  };
  const v4f z4 = {0.f, 0.f, 0.f, 0.f};
  unsigned aXSr = L + L_XS + (w < 6 ? (i >> 1) * 320 + 32 * g + 16 * (i & 1) : g * 320 + 16 * i); pin(aXSr);
  unsigned oQ = w < 6 ? ((lane & 29) == 0 ? 64 * w + 16 * g + 8 * i : OOB) : ((lane & 31) == 0 ? 384 + 4 * g : OOB); pin(oQ);
  unsigned oX2 = g < 2 ? 32 * i + 16 * g : OOB; pin(oX2);
  auto phaseQ = [&](int mode, v4f d0, v4f d1) {
    if (w < 6) {
      if (mode == 2) return;
      v4u a0 = lrd<v4u>(aXSr), a1 = lrd<v4u>(aXSr + 128);
      SB();
      d0 = smf<0>(a0, wqf[0], d0, idx); d1 = smf<0>(a0, wqf[2], d1, idx);
      d0 = smf<0>(a1, wqf[1], d0, idx); d1 = smf<0>(a1, wqf[3], d1, idx);
      SB();
      float v = rowsum2(fold(d0), fold(d1));
      unsigned qv = pk(v, dpp<X1P>(v));
      if (mode == 0) {
        const unsigned o = i < 2 ? 0x3f803f80u : 0u;
        const v4u on = {o, o, o, o};
        d0 = smf<0>(on, wqf[1], smf<0>(on, wqf[0], z4, idx), idx); d1 = smf<0>(on, wqf[3], smf<0>(on, wqf[2], z4, idx), idx);
        float c = rowsum2(fold(d0), fold(d1));
        const int d = 16 * w + 8 * (g >> 1) + (i >> 1), p = (d & ~7) | ((d & 1) << 2) | ((d >> 1) & 3);
        st4r(rW, sel((lane & 17) == 0, 16 * p), myBox + Q0, v4u{qv, pk(c, dpp<X1P>(c)), tagL, tagH});
      } else st4r(rW, oQ + Q1, myBox, col4<4>(canon(qv)));
    } else if (w == 6) {
      v4f d = smf<1>(del, cat(lrd<v4u>(aXSr), lrd<v4u>(aXSr + 1280)), d0, idx);
      if (mode == 2) { __builtin_amdgcn_raw_buffer_store_b64(v2u{pk(d[0], d[1]), pk(d[2], d[3])}, mkrs(out, 1280), oX2 >> 1, 256 * s, 0); return; }
      float sx = fold(d), sxx = fold(d * d);
      unsigned sv = __float_as_uint(add16(sum16(g >= 2 ? sx : sxx)));
      if (mode == 0) st4r(rW, lane ? OOB : 96 * 16, myBox + Q0, v4u{sv, (unsigned)__builtin_amdgcn_readlane(sv, 32), tagL, tagH});
      else {
        sv = canon(sv);
        st2r(rW, oQ + Q1, myBox, v2u{sv, sv});
        SB();
        st4r(rW, oX2 + XR2, myBox, v4u{canon(__float_as_uint(d[0])), canon(__float_as_uint(d[1])), canon(__float_as_uint(d[2])), canon(__float_as_uint(d[3]))});
      }
    }
    fence();
  };
  if (!isB) { readWq(); phaseQ(0, z4, z4); }
  else if (w == 6) st4r(rW, lane ? OOB : 96 * 16, myBox + Q0, v4u{0, 0, tagL, tagH});
  fence();
  __builtin_amdgcn_s_barrier();
  fence();
  STAMP(3);
#pragma unroll
  for (int j = 6; j < 12; ++j) kr[j] = ld4<NT>(rk, qd * 384 + u * 16 + (j / 3) * 3072 + (j % 3) * 128, kvBase);
  fence();
  const unsigned oPa = w < 5 ? lane * 16 : 96 * 16, oPb = w < 5 && lane < 32 ? 1024 + 16 * lane : 96 * 16;
  v4u pga = ld4<SC1>(rW, w >= 4 ? oPa : OOB, prtBox + Q0), pgb = ld4<SC1>(rW, w >= 4 ? oPb : OOB, prtBox + Q0);
  fence();
  v4u va[8]; v2u vb[8];
  const rsrc_t rv = mkrs(Vc, 32u * 8192 * 384);
#pragma unroll
  for (int j = 0; j < 8; ++j) { va[j] = ld4<NT>(rv, g * 1536 + i * 16 + (j & 3) * 384, kvBase + 6144 * (j >> 2)); vb[j] = ld2<NT>(rv, g * 1536 + i * 8 + 256 + (j & 3) * 384, kvBase + 6144 * (j >> 2)); }
  fence();
  v4u pgc = ld4<SC1>(rW, w < 4 ? oPa : OOB, prtBox + Q0), pgd = ld4<SC1>(rW, w < 4 ? oPb : OOB, prtBox + Q0);
  fence();
  STAMP(4);
  v8u kf[6];
#pragma unroll
  for (int h = 0; h < 2; ++h) {
#pragma unroll
    for (int j = 0; j < 6; ++j) lwr<v4u>(L + L_KS + w * 6400 + (8 * (j / 3) + qd) * 400 + (j % 3) * 128 + u * 16, kr[6 * h + j]);
    asm volatile("" ::: "memory");
#pragma unroll
    for (int c = 0; c < 3; ++c) kf[3 * h + c] = cat(lrd<v4u>(L + L_KS + w * 6400 + i * 400 + 128 * c + 16 * g), lrd<v4u>(L + L_KS + w * 6400 + i * 400 + 128 * c + 64 + 16 * g));
    asm volatile("" ::: "memory");
  }
  fence();
  STAMP(5);
  if (w < 4) { pin(pgc); pin(pgd); pga = pgc; pgb = pgd; }
  {
    int it = 0;
    while (anyl((((pga.z ^ tagL) | (pgb.z ^ tagL)) | ((pga.w ^ tagH) | (pgb.w ^ tagH))) != 0) && ++it < CAP) {
      __builtin_amdgcn_s_sleep(SL_RETRY); fence();
      pga = ld4<SC1>(rW, oPa, prtBox + Q0); pgb = ld4<SC1>(rW, oPb, prtBox + Q0);
    }
    CNT(24, it);
  }
  fence();
  lwr<unsigned>(L + L_QI + w * 448 + 4 * lane, pga.x); lwr<unsigned>(L + L_OI + w * 448 + 4 * lane, pga.y);
  lwr<unsigned>(L + L_QI + w * 448 + (lane < 32 ? 256 + 4 * lane : 384), pgb.x); lwr<unsigned>(L + w * 448 + (lane < 32 ? L_OI + 256 + 4 * lane : L_QI + 392), pgb.y);
  bar();
  if (s == 0 && w == 7) st2r(rW, lane < 8 ? lane * 256 : OOB, WS_FLAG + 8 * r, v2u{tagL, tagH});
  STAMP(6);
  unsigned aA = L + (i >> 1) * 448 + 32 * g + 16 * (i & 1); pin(aA);
  unsigned aSt = L + L_QI + u * 448 + 384; pin(aSt);
  float kc;
  {
    v4f s0 = z4, s1 = z4;
#pragma unroll
    for (int c = 0; c < 3; ++c) { v4u a = lrd<v4u>(aA + L_OI + 128 * c); s0 = smf<0>(a, kf[c], s0, idx); s1 = smf<0>(a, kf[3 + c], s1, idx); }
    kc = rowsum2(fold(s0), fold(s1));
  }
  float mw, pw; v4u pa;
  auto qk = [&](v4f s0, v4f s1) {
    v4f st = lrd<v4f>(aSt);
    v4u a[3];
#pragma unroll
    for (int c = 0; c < 3; ++c) a[c] = lrd<v4u>(aA + L_QI + 128 * c);
    SB();
    float sxx = st[0] + dpp<X1P>(st[0]), sx = st[2] + dpp<X1P>(st[2]);
    s0 = smf<0>(a[0], kf[0], s0, idx);
    SB();
    sxx += dpp<X2P>(sxx); sx += dpp<X2P>(sx);
    s0 = smf<0>(a[1], kf[1], s0, idx);
    SB();
    sxx += dpp<HMIR>(sxx); sx += dpp<HMIR>(sx);
    s0 = smf<0>(a[2], kf[2], s0, idx);
    SB();
    float mean = sx * (1.f / 640), var = fmaxf(sxx * (1.f / 640) - mean * mean, 0.f) + 1e-5f;
    s1 = smf<0>(a[0], kf[3], s1, idx);
    SB();
    float rs = __builtin_amdgcn_rsqf(var), mk = mean * kc;
    s1 = smf<0>(a[1], kf[4], s1, idx);
    SB();
    rs *= 0.07216878364870322f * 1.4426950408889634f;
    s1 = smf<0>(a[2], kf[5], s1, idx);
    SB();
    float off = rs * mk, f0 = fold(s0);
    SB();
    float sc = rowsum2(f0, fold(s1)) * rs - off;
    mw = max32(sc);
    pw = __builtin_amdgcn_exp2f(sc - mw);
    float pn = dpp<X1P>(pw);
    unsigned pp = pk(pw, pn);
    pw += pn;
    pa = v4u{dppu<BCAST + 0>(pp), dppu<BCAST + 2>(pp), dppu<BCAST + 4>(pp), dppu<BCAST + 6>(pp)};
    SB();
  };
  qk(z4, z4);
  fence();
  STAMP(7);
  v8u vf[6];
  auto permV = [&]() {
#pragma unroll
    for (int k = 0; k < 4; ++k) {
#pragma unroll
      for (int c = 0; c < 4; ++c) {
        vf[c][2 * k] = __builtin_amdgcn_perm(va[2 * k + 1][c], va[2 * k][c], 0x05040100u);
        vf[c][2 * k + 1] = __builtin_amdgcn_perm(va[2 * k + 1][c], va[2 * k][c], 0x07060302u);
      }
#pragma unroll
      for (int c = 0; c < 2; ++c) {
        vf[4 + c][2 * k] = __builtin_amdgcn_perm(vb[2 * k + 1][c], vb[2 * k][c], 0x05040100u);
        vf[4 + c][2 * k + 1] = __builtin_amdgcn_perm(vb[2 * k + 1][c], vb[2 * k][c], 0x07060302u);
      }
      SB();
    }
  };
  unsigned aOWw = L + L_OW + w * 832 + 8 * i; pin(aOWw);
  unsigned aMLw = L + L_OW + w * 832 + 768; pin(aMLw);
  unsigned aOWr = L + L_OW + u * 832; pin(aOWr);
  unsigned aOWq = aOWr + 96 * w + 16 * (qd < 6 ? qd : 0); pin(aOWq);
  unsigned oO = sel(i == 0, sel(g < 3, 48 * w + 16 * g, w == 0 ? 384 : OOB)); pin(oO);
  v4f z[6];
  auto zeroZ = [&]() {
#pragma unroll
    for (int c = 0; c < 6; ++c) z[c] = zero4();
  };
  auto pv = [&](int OO) {
    z[0] = smf<0>(pa, vf[0], z[0], idx);
    SB();
    pw += dpp<X2P>(pw);
    z[1] = smf<0>(pa, vf[1], z[1], idx);
    SB();
    pw += dpp<HMIR>(pw);
    z[2] = smf<0>(pa, vf[2], z[2], idx);
    SB();
    pw += dpp<ROR + 8>(pw);
    z[3] = smf<0>(pa, vf[3], z[3], idx);
    SB();
    pw = add32(pw);
    z[4] = smf<0>(pa, vf[4], z[4], idx);
    SB();
    lwr<v2f>(aMLw, v2f{mw, pw});
    lwr<v2f>(aOWw, v2f{z[0][0], z[0][1]}); lwr<v2f>(aOWw + 128, v2f{z[1][0], z[1][1]});
    z[5] = smf<0>(pa, vf[5], z[5], idx);
    SB();
#pragma unroll
    for (int c = 2; c < 6; ++c) lwr<v2f>(aOWw + 128 * c, v2f{z[c][0], z[c][1]});
    bar();
    v2f ml = lrd<v2f>(aOWr + 768);
    v4f ov = lrd<v4f>(aOWq);
    SB();
    float M = max8(ml.x), e = __builtin_amdgcn_exp2f(ml.x - M);
    if (g == 3) ov = v4f{0.f, 0.f, ml.y, 0.f};
    ov *= e;
    float r0, r1;
    asm("s_nop 1\n v_add_f32_dpp %0, %2, %2 row_half_mirror row_mask:0xf bank_mask:0x5\n v_add_f32_dpp %1, %3, %3 row_half_mirror row_mask:0xf bank_mask:0x5\n"
        "v_add_f32_dpp %0, %4, %4 row_half_mirror row_mask:0xf bank_mask:0xa\n v_add_f32_dpp %1, %5, %5 row_half_mirror row_mask:0xf bank_mask:0xa\n s_nop 0\n"
        "v_add_f32_dpp %0, %0, %0 quad_perm:[1,0,3,2]" DPPM "v_add_f32_dpp %1, %1, %1 quad_perm:[1,0,3,2]" DPPM "s_nop 0\n"
        "v_add_f32_dpp %0, %0, %0 quad_perm:[2,3,0,1]" DPPM "v_add_f32_dpp %1, %1, %1 quad_perm:[2,3,0,1]" DPPM
        "v_mov_b32_dpp %0, %6 quad_perm:[0,1,2,3] row_mask:0x8 bank_mask:0x5\n v_cvt_pk_bf16_f32 %1, %0, %1\n s_nop 0\n"
        "v_mov_b32_dpp %1, %0 quad_perm:[0,1,2,3] row_mask:0x8 bank_mask:0xf\n"
        : "=&v"(r0), "=&v"(r1) : "v"(ov[0]), "v"(ov[1]), "v"(ov[2]), "v"(ov[3]), "v"(M));
    st4r(rW, oO + OO, myBox, col4<4>(canon(__float_as_uint(r1))));
    fence();
  };
  unsigned oG = lane < 25 ? 16 * lane : OOB; pin(oG);
  unsigned aGw = L + w * 448 + 16 * (lane < 25 ? lane : 25); pin(aGw);
  auto rowGather = [&](v4u v, int off, int img, int cn) {
    lwr<v4u>(aGw + img, v);
    SB();
    int it = 0;
    while (anyl(mx4(v) == ~0u) && ++it < CAP) { __builtin_amdgcn_s_sleep(SL_RETRY); fence(); v = ld4<SC1>(rW, oG + off, prtBox); lwr<v4u>(aGw + img, v); SB(); }
    CNT(cn, it);
  };
  auto flagsOK = [&]() {
    int it = 0;
    for (;;) {
      fence();
      v2u f = ld2<SC1>(rW, (lane & 31) * 8, WS_FLAG + 256 * s);
      if (!anyl(((f.x ^ tagL) | (f.y ^ tagH)) != 0) || ++it >= CAP) break;
      __builtin_amdgcn_s_sleep(4);
    }
  };
  if (!isB) {
    const rsrc_t ro = mkrs(wo, 6144 * 640 * 2);
#pragma unroll
    for (int j = 0; j < 6; ++j) t[j] = ld4<NT>(ro, (tid >> 4) * 1280 + i * 16, (192 * r + 32 * j) * 1280 + s * 256);
    fence();
    permV();
    zeroZ();
    pv(O0);
    STAMP(8);
    v8u wf[3];
    unsigned aML = L + L_OI + ((2 * g + i) & 7) * 448 + 384; pin(aML);
    unsigned oXo = (lane & 55) == 0 ? 32 * w + 2 * i : OOB; pin(oXo);
    unsigned oMg = g * 8 * BOX + 16 * i; pin(oMg);
    auto phaseO = [&](int XO, v4f d) {
      v2f ml = lrd<v2f>(aML);
      v4u a[3];
#pragma unroll
      for (int c = 0; c < 3; ++c) a[c] = lrd<v4u>(aA + L_OI + 128 * c);
      SB();
#pragma unroll
      for (int c = 0; c < 3; ++c) d = smf<0>(a[c], wf[c], d, idx);
      SB();
      float e = __builtin_amdgcn_exp2f(ml.x - max8(ml.x));
      float v = dpp<BCAST + 0>(e) * (d[0] + d[1]) + dpp<BCAST + 1>(e) * (d[2] + d[3]);
      float f = __builtin_amdgcn_rcpf(sum8(e * ml.y));
      v = add32(add16(v)) * f;
      st4r(rW, oXo + XO, myBox, col4<2>(canon(pk(v, dpp<X1P>(v)))));
      fence();
    };
    auto waitInd = [&](int IO, int cn) {
      if (w == 7) {
        __builtin_amdgcn_s_sleep(SL_I);
        int it = 0;
        for (;;) {
          fence();
          unsigned f = ld1<SC1>(rW, 0, rowBox + (5 + s % 3) * BOX + IO);
          if (!anyl(f == ~0u) || ++it >= CAP) break;
        }
        CNT(cn, it);
        if (SL_M) __builtin_amdgcn_s_sleep(SL_M);
      }
      __builtin_amdgcn_s_barrier();
      fence();
    };
    auto gatherM = [&](int DO, int cn) {
      v4u a[3];
      const unsigned base = WS_BOX + (32 * w + 5) * BOX + DO + 256 * s;
#pragma unroll
      for (int q = 0; q < 3; ++q) a[q] = ld4<SC1>(rW, oMg, base + q * BOX);
      int it = 0;
      for (;;) {
        v4f d = zero4();
        SB();
        d = smf<1>(del, cat(a[0], a[1]), d, idx);
        d = smf<1>(del, cat(a[2], v4u{0, 0, 0, 0}), d, idx);
        lwr<v2u>(aXSw, v2u{pk(d[0], d[1]), pk(d[2], d[3])});
        SB();
        if (!anyl(__builtin_isunordered(d[0], d[2]))) break;
        if (!anyl(max(max(mx4(a[0]), mx4(a[1])), mx4(a[2])) == ~0u) || ++it >= CAP) break;
        __builtin_amdgcn_s_sleep(SL_RETRY); fence();
#pragma unroll
        for (int q = 0; q < 3; ++q) if (anyl(mx4(a[q]) == ~0u)) a[q] = ld4<SC1>(rW, oMg, base + q * BOX);
        fence();
      }
      CNT(cn, it);
      bar();
    };
    __builtin_amdgcn_s_sleep(SL_O0);
    v4u v = ld4<SC1>(rW, oG + O0, prtBox);
    fence();
#pragma unroll
    for (int j = 0; j < 6; ++j) { const int row = (tid >> 4) + 32 * j;
      const int d = j < 4 ? 16 * ((row >> 1) & 3) + (row >> 3) : 64 + 16 * ((row >> 1) & 1) + ((row - 128) >> 2);
      lwr<v4u>(L + L_KS + (2 * kpair(d) + (row & 1)) * 288 + 16 * i, t[j]); }
    bar();
#pragma unroll
    for (int c = 0; c < 3; ++c) wf[c] = trd16(L + L_KS + (64 * c + 8 * g + tq) * 288 + 32 * w + 8 * tp, 288);
    v4f d0 = zero4(), d1;
    fence();
    rowGather(v, O0, L_OI, 25);
    bar();
    STAMP(9);
    phaseO(D0, d0);
    STAMP(10);
    readWq();
    d0 = zero4(); d1 = zero4();
    if (w == 7) flagsOK();
    waitInd(IND0, 23);
    STAMP(11);
    gatherM(D0, 27);
    STAMP(13);
    phaseQ(1, d0, d1);
    STAMP(14);
    d0 = zero4(); d1 = zero4(); zeroZ();
    if (w < 5) { __builtin_amdgcn_s_sleep(SL_Q); rowGather(ld4<SC1>(rW, oG + Q1, prtBox), Q1, L_QI, 28); }
    bar();
    STAMP(15);
    qk(d0, d1);
    pv(O1);
    STAMP(16);
    d0 = zero4();
    __builtin_amdgcn_s_sleep(SL_O);
    rowGather(ld4<SC1>(rW, oG + O1, prtBox), O1, L_OI, 29);
    bar();
    phaseO(D1, d0);
    STAMP(17);
    if (isF) {
      d0 = zero4();
      waitInd(IND1, 21);
      gatherM(D1, 31);
      phaseQ(2, d0, d0);
      if (s == 0 && w == 7) st2r(rW, lane ? OOB : 0, 0, v2u{tagL, tagH});
      STAMP(19);
    }
    DUMP();
    __builtin_amdgcn_endpgm();
  } else {
    const rsrc_t r2 = mkrs(w2, 1536 * 640 * 2);
#pragma unroll
    for (int j = 0; j < 2; ++j) t[4 + j] = ld4<0>(r1, o1v, o1s + (64 + 32 * j) * 3072);
#pragma unroll
    for (int j = 0; j < 4; ++j) t[j] = ld4<NT>(r2, w < 5 ? 4 * g * 1280 + i * 16 : OOB, (16 * pB + j) * 1280 + w * 256);
    fence();
    permV();
    zeroZ();
    pv(O0);
    STAMP(8);
    v8u wf[4];
    float cs1, bias = lo16(b1v);
    v4f xr = {lo16(xin.x), hi16(xin.x), lo16(xin.y), hi16(xin.y)};
    unsigned oXg = g * 8 * BOX + 16 * i; pin(oXg);
    unsigned aXPr = L + L_XS + w * 256 + 32 * g + 16 * (i & 1); pin(aXPr);
    unsigned aHPw = L + L_HP + 32 * i + 4 * w; pin(aHPw);
    unsigned aS8w = L + L_S8 + 8 * w + 4 * (g >> 1); pin(aS8w);
    unsigned aHPr = L + L_HP + 32 * (8 * (g & 1) + u); pin(aHPr);
    unsigned mk = (g >> 1) == ((i >> 1) & 1) ? ~0u : 0u; pin(mk);
    unsigned oM = g == 0 ? 256 * w + 16 * i : OOB; pin(oM);
    auto phaseB = [&](int DO, int IO, int cn) {
      v4f dA, dB;
      if (w < 5) {
        v4u a[8];
        const unsigned base = WS_BOX + w * BOX + DO;
#pragma unroll
        for (int q = 0; q < 8; ++q) a[q] = ld4<SC1>(rW, oXg, base + 32 * q * BOX);
        v4f h = zero4(), d;
        v4u x0, x1;
        int it = 0;
        for (;;) {
          d = xr; pin(d);
          SB();
#pragma unroll
          for (int q = 0; q < 4; ++q) d = smf<1>(del, cat(a[2 * q], a[2 * q + 1]), d, idx);
          lwr<v2u>(aXSw, v2u{pk(d[0], d[1]), pk(d[2], d[3])});
          asm volatile("" ::: "memory");
          x0 = lrd<v4u>(aXPr); x1 = lrd<v4u>(aXPr + 128);
          SB();
          if (!anyl(__builtin_isunordered(d[0], d[2]))) break;
          unsigned m = 0;
#pragma unroll
          for (int q = 0; q < 8; ++q) m = max(m, mx4(a[q]));
          if (!anyl(m == ~0u) || ++it >= CAP) break;
          __builtin_amdgcn_s_sleep(SL_RETRY); fence();
#pragma unroll
          for (int q = 0; q < 8; ++q) if (anyl(mx4(a[q]) == ~0u)) a[q] = ld4<SC1>(rW, oXg, base + 32 * q * BOX);
          fence();
        }
        CNT(cn, it);
        float sx = fold(d), sxx = fold(d * d);
        float sv = add16(sum16(g >= 2 ? sx : sxx));
        lwr<float>(aS8w, sv);
        SB();
        h = smf<0>(x1, wf[1], smf<0>(x0, wf[0], h, idx), idx);
        SB();
        d *= 1.f / 96;
#pragma unroll
        for (int k = 0; k < 4; ++k) { auto sw = __builtin_amdgcn_permlane16_swap(__float_as_uint(d[k]), __float_as_uint(d[k]), false, false); dA[k] = __uint_as_float(sw[0]); dB[k] = __uint_as_float(sw[1]); }
        SB();
        lwr<float>(aHPw, h[0] + h[1]);
      }
      bar();
      if (w < 5) {
        v4f s0 = lrd<v4f>(L + L_S8), s1 = lrd<v4f>(L + L_S8 + 16); v2f s2 = lrd<v2f>(L + L_S8 + 32);
        v4f hp = lrd<v4f>(aHPr); float h4 = lrd<float>(aHPr + 16);
        SB();
        v2f tt = ((v2f{s0[0], s0[1]} + v2f{s0[2], s0[3]}) + (v2f{s1[0], s1[1]} + v2f{s1[2], s1[3]}) + s2) * (1.f / 640);
        float hr = fold(hp) + h4;
        float mean = tt.y, var = fmaxf(tt.x - mean * mean, 0.f);
        float hh = (hr - mean * cs1) * __builtin_amdgcn_rsqf(var + 1e-5f) + bias;
        float e = __builtin_amdgcn_exp2f(fminf(hh * 1.4426950408889634f, 50.f));
        float n = e * (e + 2.f);
        float act = (hh * n) * __builtin_amdgcn_rcpf(n + 2.f);
        unsigned pp = pk(act, dpp<X1P>(act));
        v4u bc = {dppu<BCAST + 0>(pp) & mk, dppu<BCAST + 2>(pp) & mk, dppu<BCAST + 4>(pp) & mk, dppu<BCAST + 6>(pp) & mk};
        dA = smf<0>(bc, wf[2], dA, idx);
        dB = smf<0>(bc, wf[3], dB, idx);
        st4r(rW, oM + DO, myBox, v4u{canon(pk(dA[0], dA[1])), canon(pk(dA[2], dA[3])), canon(pk(dB[0], dB[1])), canon(pk(dB[2], dB[3]))});
      } else if (w == 5) __builtin_amdgcn_raw_buffer_store_b32(0u, rW, lane ? OOB : IO, myBox, SC1);
      fence();
    };
    {
#pragma unroll
      for (int j = 0; j < 2; ++j) lwr<v4u>(a1w + 2048 + 1024 * j, t[4 + j]);
      asm volatile("" ::: "memory");
#pragma unroll
      for (int c = 0; c < 2; ++c) wf[c] = trd16(L + L_WQ + w * 4096 + (64 * c + 8 * g + tq) * 32 + 8 * tp, 32);
#pragma unroll
      for (int c = 0; c < 2; ++c)
#pragma unroll
        for (int hf = 0; hf < 2; ++hf)
#pragma unroll
          for (int k = 0; k < 2; ++k) {
            wf[2 + c][4 * hf + 2 * k] = __builtin_amdgcn_perm(t[1 + 2 * k][2 * c + hf], t[2 * k][2 * c + hf], 0x05040100u);
            wf[2 + c][4 * hf + 2 * k + 1] = __builtin_amdgcn_perm(t[1 + 2 * k][2 * c + hf], t[2 * k][2 * c + hf], 0x07060302u);
          }
      const v4u on = {0x3f803f80u, 0x3f803f80u, 0x3f803f80u, 0x3f803f80u};
      v4f h = smf<0>(on, wf[1], smf<0>(on, wf[0], z4, idx), idx);
      lwr<float>(aHPw, h[0] + h[1]);
      if (w == 7) flagsOK();
    }
    bar();
    cs1 = fold(lrd<v4f>(aHPr)) + lrd<float>(aHPr + 16);
    bar();
    STAMP(11);
    __builtin_amdgcn_s_sleep(SL_X0);
    phaseB(D0, IND0, 26);
    STAMP(12);
    v4f d0 = zero4(), d1 = zero4(); zeroZ();
    if (w < 5) { __builtin_amdgcn_s_sleep(SL_QB); rowGather(ld4<SC1>(rW, oG + Q1, prtBox), Q1, L_QI, 28); }
    bar();
    STAMP(15);
    qk(d0, d1);
    pv(O1);
    STAMP(16);
    if (w < 5) {
      v4u v = ld4<SC1>(rW, 32 * i + 16 * (g & 1), prtBox + XR2);
      fence();
      __builtin_amdgcn_s_sleep(SL_X1);
      int it = 0;
      while (anyl(mx4(v) == ~0u) && ++it < CAP) { fence(); v = ld4<SC1>(rW, 32 * i + 16 * (g & 1), prtBox + XR2); }
      xr = __builtin_bit_cast(v4f, v);
    }
    phaseB(D1, IND1, 30);
    STAMP(18);
  }
  DUMP();
}

extern "C" void kernel_launch(void* const* d_in, const int* in_sizes, int n_in, void* d_out, int out_size, void* d_ws, size_t ws_size, hipStream_t stream) {
  k_fused<<<256, 512, 0, stream>>>(d_in[0], d_in[1], d_in[2], d_in[3], d_in[4], d_in[6], d_in[7], d_in[8], d_out, (char*)d_ws);
}
```

```cpp
#include <hip/hip_runtime.h>
typedef unsigned v16u __attribute__((ext_vector_type(16)));
typedef unsigned v8u __attribute__((ext_vector_type(8)));
typedef unsigned v4u __attribute__((ext_vector_type(4)));
typedef unsigned v2u __attribute__((ext_vector_type(2)));
typedef float v4f __attribute__((ext_vector_type(4)));
typedef float v2f __attribute__((ext_vector_type(2)));
typedef short v4s __attribute__((ext_vector_type(4)));
typedef __bf16 v16b __attribute__((ext_vector_type(16)));
typedef __bf16 v8b __attribute__((ext_vector_type(8)));
typedef __bf16 v2b __attribute__((ext_vector_type(2)));
typedef unsigned long long u64;
typedef __amdgpu_buffer_rsrc_t rsrc_t;
#define LAS __attribute__((address_space(3)))
#define DEV __device__ __forceinline__
#define INL __attribute__((always_inline))
#define RARE(c) __builtin_expect(c, 0)

#define SCHED 198,329,430,553,635,763,100,24
#define CODELEN 29300
#define NB 2
constexpr int SCH[8] = {SCHED};
constexpr int C_X0 = SCH[0], C_M0 = SCH[1], C_Q = SCH[2], C_O = SCH[3], C_X = SCH[4], C_M1 = SCH[5], SLOT = SCH[6], STR = SCH[7];
#define SL_O0 2
#define SL_RETRY 1
#define SL_P 3
#define NA 6
#define PP 6
constexpr int CAP = 40000;

constexpr int WS_BOX = 4096, BOX = 51 * 128;
constexpr int Q1 = 0, O0 = 512, O1 = 1024, XR2 = 1536, D0 = 2048, D1 = 3328, CLR = 4736;
constexpr int HINT = CLR - 128;
constexpr int Q0 = CLR;
constexpr int WS_END = WS_BOX + 256 * BOX;
constexpr unsigned OOB = 0x7fff0000u;
constexpr int NT = 2, SC1 = 16;

constexpr int L_QI = 0;
constexpr int L_OI = L_QI + 8 * 448;
constexpr int L_OW = L_OI + 16 * 448;
constexpr int L_XS = L_OW + 8 * 832;
constexpr int L_HP = L_XS + 8 * 320;
constexpr int L_S8 = L_HP + 1024;
constexpr int L_T = L_S8 + 48;
constexpr int L_KS = L_S8 + 64;
constexpr int L_WQ = L_KS + 192 * 288;
constexpr int L_END = L_WQ + 128 * 416;

DEV rsrc_t mkrs(const void* p, unsigned bytes) { return __builtin_amdgcn_make_buffer_rsrc((void*)p, (short)0, (int)bytes, 0x00020000); }
template <int AUX> DEV v4u ld4(rsrc_t r, unsigned vo, unsigned so = 0) { return __builtin_amdgcn_raw_buffer_load_b128(r, vo, so, AUX); }
template <int AUX> DEV v2u ld2(rsrc_t r, unsigned vo, unsigned so = 0) { return __builtin_amdgcn_raw_buffer_load_b64(r, vo, so, AUX); }
template <int AUX> DEV unsigned ld1(rsrc_t r, unsigned vo, unsigned so = 0) { return __builtin_amdgcn_raw_buffer_load_b32(r, vo, so, AUX); }
DEV unsigned canon(unsigned v) { return min(v, 0xfffffffeu); }
DEV void st2r(rsrc_t r, unsigned vo, unsigned so, v2u v) { __builtin_amdgcn_raw_buffer_store_b64(v, r, vo, so, SC1); }
DEV void st4r(rsrc_t r, unsigned vo, unsigned so, v4u v) { __builtin_amdgcn_raw_buffer_store_b128(v, r, vo, so, SC1); }
DEV void SB() { __builtin_amdgcn_sched_barrier(0); }
DEV void fence() { asm volatile("" ::: "memory"); SB(); }
DEV void bar() { asm volatile("s_waitcnt lgkmcnt(0)" ::: "memory"); __builtin_amdgcn_s_barrier(); fence(); }
template <class T> DEV void pin(T& v) { asm volatile("" : "+v"(v)); }
DEV int zs() { int z = 0; asm volatile("" : "+s"(z)); return z; }
DEV v4f zero4() { v4f z = {0.f, 0.f, 0.f, 0.f}; pin(z); return z; }
template <int AB> DEV v4f smf(v4u a, v8u b, v4f c, unsigned idx) { return __builtin_amdgcn_smfmac_f32_16x16x64_bf16(__builtin_bit_cast(v8b, a), __builtin_bit_cast(v16b, b), c, idx, 0, AB); }
DEV v8u cat(v4u a, v4u b) { return __builtin_shufflevector(a, b, 0, 1, 2, 3, 4, 5, 6, 7); }
DEV unsigned pk(float a, float b) { v2f v = {a, b}; return __builtin_bit_cast(unsigned, __builtin_convertvector(v, v2b)); }
DEV float lo16(unsigned v) { return __uint_as_float(v << 16); }
DEV float hi16(unsigned v) { return __uint_as_float(v & 0xffff0000u); }
template <int C> DEV float dpp(float x) { return __uint_as_float(__builtin_amdgcn_update_dpp(0u, __float_as_uint(x), C, 0xf, 0xf, true)); }
template <int C> DEV unsigned dppu(unsigned x) { return __builtin_amdgcn_update_dpp(0u, x, C, 0xf, 0xf, true); }
constexpr int X1P = 0xB1, X2P = 0x4E, HMIR = 0x141, SHL = 0x100, ROR = 0x120, BCAST = 0x150;
DEV float sum8(float x) { x += dpp<X1P>(x); x += dpp<X2P>(x); x += dpp<HMIR>(x); return x; }
DEV float sum16(float x) { x = sum8(x); x += dpp<ROR + 8>(x); return x; }
template <int S> DEV v4u col4(unsigned p) { return v4u{p, dppu<SHL + S>(p), dppu<SHL + 2 * S>(p), dppu<SHL + 3 * S>(p)}; }
#define DPPM " row_mask:0xf bank_mask:0xf\n"
DEV float max8(float x) { float m;
  asm("s_nop 1\n v_max_f32_dpp %0, %1, %1 quad_perm:[1,0,3,2]" DPPM "s_nop 1\n v_max_f32_dpp %0, %0, %0 quad_perm:[2,3,0,1]" DPPM
      "s_nop 1\n v_max_f32_dpp %0, %0, %0 row_half_mirror" DPPM : "=&v"(m) : "v"(x)); return m; }
DEV float max32(float& x) { float m, t;
  asm("s_nop 1\n v_max_f32_dpp %0, %2, %2 quad_perm:[1,0,3,2]" DPPM "v_mov_b32_dpp %2, %2 row_ror:8 row_mask:0xa bank_mask:0xf\n s_nop 0\n v_max_f32_dpp %0, %0, %0 quad_perm:[2,3,0,1]" DPPM
      "s_nop 1\n v_max_f32_dpp %0, %0, %0 row_half_mirror" DPPM "s_nop 1\n v_max_f32_dpp %1, %0, %0 row_mirror" DPPM "v_max_f32_dpp %0, %0, %0 row_mirror" DPPM
      "s_nop 1\n v_permlane32_swap_b32 %0, %1\n s_nop 0\n v_max_f32 %0, %0, %1" : "=&v"(m), "=&v"(t), "+v"(x)); return m; }
DEV float add16(float x) { auto s = __builtin_amdgcn_permlane16_swap(__float_as_uint(x), __float_as_uint(x), false, false); return __uint_as_float(s[0]) + __uint_as_float(s[1]); }
DEV float add32(float x) { auto s = __builtin_amdgcn_permlane32_swap(__float_as_uint(x), __float_as_uint(x), false, false); return __uint_as_float(s[0]) + __uint_as_float(s[1]); }
DEV float fold(v4f d) { v2f p = v2f{d[0], d[1]} + v2f{d[2], d[3]}; return p.x + p.y; }
DEV float rowsum2(float a, float c) {
  auto sw = __builtin_amdgcn_permlane32_swap(__float_as_uint(a), __float_as_uint(c), false, false);
  return add16(__uint_as_float(sw[0]) + __uint_as_float(sw[1]));
}
DEV unsigned mx4(v4u v) { return max(max(v.x, v.y), max(v.z, v.w)); }
DEV unsigned sel(bool c, unsigned a, unsigned b = OOB) { return c ? a : b; }
DEV unsigned now() { return (unsigned)__builtin_amdgcn_s_memrealtime(); }
DEV bool anyl(bool c) { return __builtin_amdgcn_ballot_w64(c) != 0; }
template <class T> DEV T lrd(unsigned a) { return *(LAS T*)(a); }
template <class T> DEV void lwr(unsigned a, T v) { *(LAS T*)(a) = v; }
DEV v2u trd(unsigned a) { return __builtin_bit_cast(v2u, __builtin_amdgcn_ds_read_tr16_b64_v4i16((LAS v4s*)(a))); }
DEV v8u trd16(unsigned a, int rs) { v2u p = trd(a), q = trd(a + 4 * rs), r = trd(a + 32 * rs), s = trd(a + 36 * rs); return v8u{p.x, p.y, q.x, q.y, r.x, r.y, s.x, s.y}; }
template <bool B> struct Half { static constexpr bool v = B; };
DEV int kpair(int d) { return (d & ~7) | ((d & 3) << 1) | ((d >> 2) & 1); }

__global__ void __launch_bounds__(512, 2)
k_fused(const void* x, const void* Kc, const void* Vc, const void* wq, const void* wo, const void* w1, const void* b1, const void* w2, void* out, char* ws) {
  __shared__ __attribute__((aligned(16))) char smem[L_END];
  const unsigned L = (unsigned)(size_t)(LAS char*)smem;
  const int b = blockIdx.x, tid = threadIdx.x, lane = tid & 63, w = __builtin_amdgcn_readfirstlane(tid >> 6);
  const int xcd = b & 7, bi = b >> 3, r = xcd * 4 + (bi >> 3), s = bi & 7;
  const bool isB = s >= 5, isF = r == 3;
  const int pB = 3 * r + s - 5;
  const int g = lane >> 4, i = lane & 15, u = lane & 7, qd = lane >> 3, tq = i >> 2, tp = i & 3;
  u64 pc0;
  asm volatile("s_getpc_b64 %0" : "=s"(pc0));
  unsigned pf = ld1<0>(mkrs((const void*)pc0, CODELEN), sel(isB && tid * 128 < CODELEN, tid * 128));
  const rsrc_t rW = mkrs(ws, WS_END);
  const unsigned rowBox = WS_BOX + r * 8 * BOX, myBox = rowBox + s * BOX, prtBox = rowBox + w * BOX;
  const u64 epoch = *(const __attribute__((address_space(4))) u64*)ws + 1;
  unsigned tagL = (unsigned)epoch, tagH = (unsigned)(epoch >> 32);
  st4r(rW, lane < CLR / 128 ? lane * 16 : OOB, myBox + w * (CLR / 8), v4u{~0u, ~0u, ~0u, ~0u});
  fence();
  v4u t[6];
  if (!isB) {
    const rsrc_t rq = mkrs(wq, 640 * 6144 * 2);
#pragma unroll
    for (int j = 0; j < 6; ++j) { int ch = j * 512 + tid; t[j] = ld4<NT>(rq, (ch / 24) * 12288 + (ch % 24) * 16, 128 * s * 12288 + r * 384); }
  }
  v2u xin = ld2<0>(mkrs(x, 1280), (isB || w == 0) ? 16 * i + 8 * (g & 1) : OOB, 256 * (isB ? w : s));
  fence();
  __builtin_amdgcn_s_barrier();
  fence();
  const unsigned kvBase = ((unsigned)r * 8192 + 6144 + s * 256 + w * 32) * 384;
  const rsrc_t rk = mkrs(Kc, 32u * 8192 * 384);
  v4u kr[12];
#pragma unroll
  for (int j = 0; j < 6; ++j) kr[j] = ld4<NT>(rk, qd * 384 + u * 16 + (j / 3) * 3072 + (j % 3) * 128, kvBase);
  fence();
  asm volatile("s_waitcnt vmcnt(6)" ::: "memory");
  pin(pf);
  unsigned aXSw = L + L_XS + w * (isB ? 256 : 320) + 16 * i + 8 * (g & 1); pin(aXSw);
  if (!isB) {
#pragma unroll
    for (int j = 0; j < 6; ++j) { int ch = j * 512 + tid, row = ch / 24; lwr<v4u>(L + L_WQ + (2 * kpair(row >> 1) + (row & 1)) * 416 + (ch % 24) * 16, t[j]); }
    lwr<v2u>(aXSw, xin);
  }
  asm volatile("" : "+s"(tagL), "+s"(tagH));
  bar();
  const unsigned idx = ((i & 1) ? 0xEEEEu : 0x4444u) | ((i & 2) ? 0xEEEE0000u : 0x44440000u);
  v4u del;
  { const unsigned o = (i & 1) ? 0x3f800000u : 0x3f80u; del = (i & 4) ? v4u{0, o, 0, o} : v4u{o, 0, o, 0}; }
  v8u wqf[4];
  auto readWq = [&]() INL {
    const unsigned a = L + L_WQ + 64 * (w < 6 ? w : 0) + 8 * tp + (8 * g + tq) * 416;
#pragma unroll
    for (int n = 0; n < 4; ++n) wqf[n] = trd16(a + 32 * (n >> 1) + 64 * 416 * (n & 1), 416);
  };
  const v4f z4 = {0.f, 0.f, 0.f, 0.f};
  unsigned aXSr = L + L_XS + (w < 6 ? (i >> 1) * 320 + 32 * g + 16 * (i & 1) : g * 320 + 16 * i); pin(aXSr);
  unsigned oQ = w < 6 ? ((lane & 29) == 0 ? 64 * w + 16 * g + 8 * i : OOB) : ((lane & 31) == 0 ? 384 + 4 * g : OOB); pin(oQ);
  unsigned oX2 = g < 2 ? 32 * i + 16 * g : OOB; pin(oX2);
  auto phaseQ = [&](int mode, v4f d0, v4f d1) INL {
    if (w < 6) {
      if (mode == 2) return;
      v4u a0 = lrd<v4u>(aXSr), a1 = lrd<v4u>(aXSr + 128);
      SB();
      d0 = smf<0>(a0, wqf[0], d0, idx); d1 = smf<0>(a0, wqf[2], d1, idx);
      d0 = smf<0>(a1, wqf[1], d0, idx); d1 = smf<0>(a1, wqf[3], d1, idx);
      SB();
      float v = rowsum2(fold(d0), fold(d1));
      unsigned qv = pk(v, dpp<X1P>(v));
      if (mode == 0) {
        const unsigned o = i < 2 ? 0x3f803f80u : 0u;
        const v4u on = {o, o, o, o};
        d0 = smf<0>(on, wqf[1], smf<0>(on, wqf[0], z4, idx), idx); d1 = smf<0>(on, wqf[3], smf<0>(on, wqf[2], z4, idx), idx);
        float c = rowsum2(fold(d0), fold(d1));
        const int d = 16 * w + 8 * (g >> 1) + (i >> 1), p = (d & ~7) | ((d & 1) << 2) | ((d >> 1) & 3);
        st4r(rW, sel((lane & 17) == 0, 16 * p), myBox + Q0, v4u{qv, pk(c, dpp<X1P>(c)), tagL, tagH});
      } else st4r(rW, oQ + Q1, myBox, col4<4>(canon(qv)));
    } else if (w == 6) {
      v4f d = smf<1>(del, cat(lrd<v4u>(aXSr), lrd<v4u>(aXSr + 1280)), d0, idx);
      if (mode == 2) { __builtin_amdgcn_raw_buffer_store_b64(v2u{pk(d[0], d[1]), pk(d[2], d[3])}, mkrs(out, 1280), oX2 >> 1, 256 * s, SC1); return; }
      float sx = fold(d), sxx = fold(d * d);
      unsigned sv = __float_as_uint(add16(sum16(g >= 2 ? sx : sxx)));
      if (mode == 0) st4r(rW, lane ? OOB : 96 * 16, myBox + Q0, v4u{sv, (unsigned)__builtin_amdgcn_readlane(sv, 32), tagL, tagH});
      else {
        sv = canon(sv);
        st2r(rW, oQ + Q1, myBox, v2u{sv, sv});
        SB();
        st4r(rW, oX2 + XR2, myBox, v4u{canon(__float_as_uint(d[0])), canon(__float_as_uint(d[1])), canon(__float_as_uint(d[2])), canon(__float_as_uint(d[3]))});
      }
    }
    fence();
  };
  if (!isB) { readWq(); phaseQ(0, z4, z4); }
  else if (w == 6) st4r(rW, lane ? OOB : 96 * 16, myBox + Q0, v4u{0, 0, tagL, tagH});
  fence();
  __builtin_amdgcn_s_barrier();
  fence();
#pragma unroll
  for (int j = 6; j < 12; ++j) kr[j] = ld4<NT>(rk, qd * 384 + u * 16 + (j / 3) * 3072 + (j % 3) * 128, kvBase);
  fence();
  __builtin_amdgcn_s_barrier();
  fence();
  const unsigned oPc = lane * 16, oPd = lane < 32 ? 1024 + 16 * lane : 96 * 16;
  const unsigned oPa = w > 0 ? 96 * 16 : oPc, oPb = w > 0 ? 96 * 16 : oPd;
  v4u pga, pgb, pgc, pgd;
  v2u pge = {tagL, tagH};
  auto pollQ = [&](bool ldo) INL { if (ldo) pge = ld2<SC1>(rW, u * BOX + 96 * 16 + 8, WS_BOX + (r ^ 1) * 8 * BOX + Q0); pga = ld4<SC1>(rW, oPa, prtBox + Q0 + 4 * BOX); pgb = ld4<SC1>(rW, oPb, prtBox + Q0 + 4 * BOX); pgc = ld4<SC1>(rW, oPc, prtBox + Q0); pgd = ld4<SC1>(rW, oPd, prtBox + Q0); };
  auto noQ = [&]() INL { return anyl((((pga.z ^ tagL) | (pgb.z ^ tagL)) | ((pga.w ^ tagH) | (pgb.w ^ tagH)) | ((pgc.z ^ tagL) | (pgd.z ^ tagL)) | ((pgc.w ^ tagH) | (pgd.w ^ tagH)) | ((pge.x ^ tagL) | (pge.y ^ tagH))) != 0); };
  v4u va[8]; v2u vb[8];
  const rsrc_t rv = mkrs(Vc, 32u * 8192 * 384);
  auto loadV = [&](int j0, int j1) INL {
#pragma unroll
    for (int j = j0; j < j1; ++j) { va[j] = ld4<NT>(rv, g * 1536 + i * 16 + (j & 3) * 384, kvBase + 6144 * (j >> 2)); vb[j] = ld2<NT>(rv, g * 1536 + i * 8 + 256 + (j & 3) * 384, kvBase + 6144 * (j >> 2)); }
    fence();
  };
  v8u vf[6];
  auto permV = [&]() INL {
#pragma unroll
    for (int k = 0; k < 4; ++k) {
#pragma unroll
      for (int c = 0; c < 4; ++c) {
        vf[c][2 * k] = __builtin_amdgcn_perm(va[2 * k + 1][c], va[2 * k][c], 0x05040100u);
        vf[c][2 * k + 1] = __builtin_amdgcn_perm(va[2 * k + 1][c], va[2 * k][c], 0x07060302u);
      }
#pragma unroll
      for (int c = 0; c < 2; ++c) {
        vf[4 + c][2 * k] = __builtin_amdgcn_perm(vb[2 * k + 1][c], vb[2 * k][c], 0x05040100u);
        vf[4 + c][2 * k + 1] = __builtin_amdgcn_perm(vb[2 * k + 1][c], vb[2 * k][c], 0x07060302u);
      }
      SB();
    }
#pragma unroll
    for (int c = 0; c < 6; ++c) pin(vf[c]);
  };
  v8u kf[6];
  auto permK = [&]() INL {
#pragma unroll
    for (int h = 0; h < 2; ++h) {
#pragma unroll
      for (int j = 0; j < 6; ++j) lwr<v4u>(L + L_KS + w * 6400 + (8 * (j / 3) + qd) * 400 + (j % 3) * 128 + u * 16, kr[6 * h + j]);
      asm volatile("" ::: "memory");
#pragma unroll
      for (int c = 0; c < 3; ++c) kf[3 * h + c] = cat(lrd<v4u>(L + L_KS + w * 6400 + i * 400 + 128 * c + 16 * g), lrd<v4u>(L + L_KS + w * 6400 + i * 400 + 128 * c + 64 + 16 * g));
      asm volatile("" ::: "memory");
    }
    fence();
  };
  unsigned aA = L + (i >> 1) * 448 + 32 * g + 16 * (i & 1); pin(aA);
  unsigned aSt = L + L_QI + u * 448 + 384; pin(aSt);
  float kc;
  float mw, pw; v4u pa;
  auto soft = [&](float sc) INL {
    mw = max32(sc);
    pw = __builtin_amdgcn_exp2f(sc - mw);
    float pn = dpp<X1P>(pw);
    unsigned pp = pk(pw, pn);
    pw += pn;
    pa = v4u{dppu<BCAST + 0>(pp), dppu<BCAST + 2>(pp), dppu<BCAST + 4>(pp), dppu<BCAST + 6>(pp)};
    SB();
  };
  auto qk = [&](v4f s0, v4f s1) INL {
    v4f st = lrd<v4f>(aSt);
    v4u a[3];
#pragma unroll
    for (int c = 0; c < 3; ++c) a[c] = lrd<v4u>(aA + L_QI + 128 * c);
    SB();
    float sxx = st[0] + dpp<X1P>(st[0]), sx = st[2] + dpp<X1P>(st[2]);
    s0 = smf<0>(a[0], kf[0], s0, idx);
    SB();
    sxx += dpp<X2P>(sxx); sx += dpp<X2P>(sx);
    s0 = smf<0>(a[1], kf[1], s0, idx);
    SB();
    sxx += dpp<HMIR>(sxx); sx += dpp<HMIR>(sx);
    s0 = smf<0>(a[2], kf[2], s0, idx);
    SB();
    float mean = sx * (1.f / 640), var = fmaxf(sxx * (1.f / 640) - mean * mean, 0.f) + 1e-5f;
    s1 = smf<0>(a[0], kf[3], s1, idx);
    SB();
    float rs = __builtin_amdgcn_rsqf(var), mk = mean * kc;
    s1 = smf<0>(a[1], kf[4], s1, idx);
    SB();
    rs *= 0.07216878364870322f * 1.4426950408889634f;
    s1 = smf<0>(a[2], kf[5], s1, idx);
    SB();
    float off = rs * mk, f0 = fold(s0);
    SB();
    float sc = rowsum2(f0, fold(s1)) * rs - off;
    soft(sc);
  };
  auto qk1 = [&]() INL {
    v4f st = lrd<v4f>(aSt), s0 = z4, s1 = z4, c0 = z4, c1 = z4;
#pragma unroll
    for (int c = 0; c < 3; ++c) {
      v4u a = lrd<v4u>(aA + L_QI + 128 * c), e = lrd<v4u>(aA + L_OI + 128 * c);
      s0 = smf<0>(a, kf[c], s0, idx); s1 = smf<0>(a, kf[3 + c], s1, idx); c0 = smf<0>(e, kf[c], c0, idx); c1 = smf<0>(e, kf[3 + c], c1, idx);
    }
    float mean = sum8(st[2]) * (1.f / 640), var = fmaxf(sum8(st[0]) * (1.f / 640) - mean * mean, 0.f) + 1e-5f;
    float rs = __builtin_amdgcn_rsqf(var) * (0.07216878364870322f * 1.4426950408889634f);
    kc = rowsum2(fold(c0), fold(c1));
    soft(rowsum2(fold(s0), fold(s1)) * rs - rs * (mean * kc));
  };
  const rsrc_t ro = mkrs(wo, 6144 * 640 * 2);
  const unsigned oWo = ((tid >> 3) & 31) * 1280 + u * 16, sWo = 192 * (r & ~1) * 1280 + (5 * (r & 1) + s) * 128;
  v4u tw[NA > 6 ? NA : 12 - NA];
  const rsrc_t r1 = mkrs(w1, 640 * 1536 * 2);
  auto loadW1 = [&](int j0, int j1) INL {
#pragma unroll
    for (int j = j0; j < j1; ++j) tw[j] = ld4<0>(r1, sel(w < 5, (lane >> 1) * 3072 + (lane & 1) * 16), (128 * w + 32 * j) * 3072 + pB * 32);
  };
  auto loadW2 = [&]() INL {
    const rsrc_t r2 = mkrs(w2, 1536 * 640 * 2);
#pragma unroll
    for (int j = 0; j < 4; ++j) t[j] = ld4<NT>(r2, sel(w < 5, 4 * g * 1280 + i * 16), (16 * pB + j) * 1280 + w * 256);
  };
  auto front = [&](bool ldo, bool lo) INL {
    if (lo) {
      loadV(0, PP);
      pollQ(ldo);
      fence();
      loadV(PP, 8);
      permK();
      if (RARE(noQ())) {
        int it = 0;
        do { __builtin_amdgcn_s_sleep(SL_RETRY); fence(); pollQ(ldo); asm volatile("s_waitcnt vmcnt(0)" ::: "memory"); } while (noQ() && ++it < CAP);
      }
      fence();
      const unsigned ai = L + w * 448 + 4 * lane, aj = L + L_QI + w * 448 + (lane < 32 ? 256 + 4 * lane : 384), ak = L + w * 448 + (lane < 32 ? L_OI + 256 + 4 * lane : L_QI + 392);
      lwr<unsigned>(ai + L_QI + 4 * 448, pga.x); lwr<unsigned>(ai + L_OI + 4 * 448, pga.y); lwr<unsigned>(aj + 4 * 448, pgb.x); lwr<unsigned>(ak + 4 * 448, pgb.y);
      lwr<unsigned>(ai + L_QI, pgc.x); lwr<unsigned>(ai + L_OI, pgc.y); lwr<unsigned>(aj, pgd.x); lwr<unsigned>(ak, pgd.y);
      bar();
      if (ldo) {
#pragma unroll
        for (int j = 0; j < NA; ++j) tw[j] = ld4<NT>(ro, oWo, sWo + 32 * j * 1280);
      } else loadW1(0, NB);
      fence();
      qk1();
      fence();
    } else {
      permK();
      loadV(0, 8);
      bar();
      qk1();
      fence();
      if (ldo) {
#pragma unroll
        for (int j = NA; j < 12; ++j) tw[j - NA] = ld4<NT>(ro, oWo, sWo + 32 * j * 1280);
      } else loadW1(0, NB);
      fence();
    }
    permV();
    fence();
  };
  unsigned aOWw = L + L_OW + w * 832 + 8 * i; pin(aOWw);
  unsigned aMLw = L + L_OW + w * 832 + 768; pin(aMLw);
  unsigned aOWr = L + L_OW + u * 832; pin(aOWr);
  unsigned aOWq = aOWr + 192 * (w & 3) + 32 * (qd < 6 ? qd : 0); pin(aOWq);
  unsigned oO = sel(u == 0 && (w < 4 || w == 7), (w < 4 ? 128 * w : HINT - O0) + 16 * qd); pin(oO);
  v4f z[6];
  auto zeroZ = [&]() INL {
#pragma unroll
    for (int c = 0; c < 6; ++c) z[c] = zero4();
  };
  auto pv = [&](int OO) INL {
    z[0] = smf<0>(pa, vf[0], z[0], idx);
    SB();
    pw += dpp<X2P>(pw);
    z[1] = smf<0>(pa, vf[1], z[1], idx);
    SB();
    pw += dpp<HMIR>(pw);
    z[2] = smf<0>(pa, vf[2], z[2], idx);
    SB();
    pw += dpp<ROR + 8>(pw);
    z[3] = smf<0>(pa, vf[3], z[3], idx);
    SB();
    pw = add32(pw);
    z[4] = smf<0>(pa, vf[4], z[4], idx);
    SB();
    lwr<v2f>(aMLw, v2f{mw, pw});
    lwr<v2f>(aOWw, v2f{z[0][0], z[0][1]}); lwr<v2f>(aOWw + 128, v2f{z[1][0], z[1][1]});
    z[5] = smf<0>(pa, vf[5], z[5], idx);
    SB();
#pragma unroll
    for (int c = 2; c < 6; ++c) lwr<v2f>(aOWw + 128 * c, v2f{z[c][0], z[c][1]});
    bar();
    v4u ov;
    if (w < 4) {
      v2f ml = lrd<v2f>(aOWr + 768);
      v4f oa = lrd<v4f>(aOWq), ob = lrd<v4f>(aOWq + 16);
      SB();
      float M = max8(ml.x), e = __builtin_amdgcn_exp2f(ml.x - M);
      if (g == 3) oa[1] = ml.y;
      oa *= e; ob *= e;
      float r0, r1, r2, r3;
#define HM(d, s, m) "v_add_f32_dpp " d ", " s ", " s " row_half_mirror row_mask:0xf bank_mask:" m "\n"
#define QP(d, p) "v_add_f32_dpp " d ", " d ", " d " quad_perm:" p DPPM
      asm("s_nop 1\n" HM("%0", "%4", "0x5") HM("%1", "%5", "0x5") HM("%2", "%6", "0x5") HM("%3", "%7", "0x5")
          HM("%0", "%8", "0xa") HM("%1", "%9", "0xa") HM("%2", "%10", "0xa") HM("%3", "%11", "0xa")
          QP("%0", "[1,0,3,2]") QP("%1", "[1,0,3,2]") QP("%2", "[1,0,3,2]") QP("%3", "[1,0,3,2]")
          QP("%0", "[2,3,0,1]") QP("%1", "[2,3,0,1]") QP("%2", "[2,3,0,1]") QP("%3", "[2,3,0,1]")
          "v_cvt_pk_bf16_f32 %0, %0, %1\n v_cvt_pk_bf16_f32 %2, %2, %3\n"
          "v_mov_b32_dpp %0, %12 quad_perm:[0,1,2,3] row_mask:0x8 bank_mask:0xf\n v_mov_b32_dpp %2, %1 quad_perm:[0,1,2,3] row_mask:0x8 bank_mask:0xf\n"
          "v_min_u32 %0, -2, %0\n v_min_u32 %2, -2, %2\n s_nop 0\n"
          : "=&v"(r0), "=&v"(r1), "=&v"(r2), "=&v"(r3) : "v"(oa[0]), "v"(oa[1]), "v"(oa[2]), "v"(oa[3]), "v"(ob[0]), "v"(ob[1]), "v"(ob[2]), "v"(ob[3]), "v"(M));
      const unsigned p0 = __float_as_uint(r0), p1 = __float_as_uint(r2);
      ov = v4u{p0, p1, dppu<SHL + 4>(p0), dppu<SHL + 4>(p1)};
      if (OO != O0) st4r(rW, oO + OO, myBox, ov);
    } else if (OO == O0) ov = v4u{now(), 1, tagL, tagH};
    if (OO == O0) { pin(ov); st4r(rW, oO + OO, myBox, ov); }
    fence();
  };
  unsigned oG = lane < 25 ? 16 * lane : OOB; pin(oG);
  unsigned aGw = L + w * 448 + 16 * (lane < 25 ? lane : 25); pin(aGw);
  unsigned T = 0;
  auto waitT = [&](int cn) INL {
    if (w == 7) {
      int it = 0;
      unsigned o[4], tm[4] = {0, 0, 0, 0};
#pragma unroll
      for (int j = 0; j < 4; ++j) o[j] = BOX * (64 * j + lane);
      for (;;) {
        fence();
        v4u f[4];
#pragma unroll
        for (int j = 0; j < 4; ++j) f[j] = ld4<SC1>(rW, o[j], WS_BOX + HINT);
#pragma unroll
        for (int j = 0; j < 4; ++j) { const bool ok = ((f[j].z ^ tagL) | (f[j].w ^ tagH) | (f[j].y ^ 1)) == 0; o[j] = sel(ok, OOB, o[j]); tm[j] = sel(ok, f[j].x, tm[j]); }
        if (!anyl(min(min(o[0], o[1]), min(o[2], o[3])) != OOB) || ++it >= CAP) break;
      }
      const unsigned t = now();
      unsigned m = min(min(t - tm[0], t - tm[1]), min(t - tm[2], t - tm[3]));
      m = min(m, dppu<X1P>(m)); m = min(m, dppu<X2P>(m)); m = min(m, dppu<HMIR>(m)); m = min(m, dppu<ROR + 8>(m));
      m = min(min((unsigned)__builtin_amdgcn_readlane(m, 0), (unsigned)__builtin_amdgcn_readlane(m, 16)), min((unsigned)__builtin_amdgcn_readlane(m, 32), (unsigned)__builtin_amdgcn_readlane(m, 48)));
      lwr<v4u>(L + L_T, v4u{t - min(m, 1000u), 0, 0, 0});
    }
    fence();
  };
  unsigned T0;
  auto readT = [&]() INL { T0 = T = __builtin_amdgcn_readfirstlane(lrd<unsigned>(L + L_T)); };
  auto until = [&](int c) INL { fence(); unsigned e;
    asm volatile("s_sub_u32 %0, %1, %2\n s_min_u32 %0, %0, 0x3e8\n s_mul_i32 %0, %0, %3\n s_lshr_b32 %0, %0, 16\n s_add_u32 %0, %0, %1" : "=&s"(e) : "s"(T), "s"(T0), "s"(c * STR) : "scc");
    e += c;
    while ((int)(now() - e) < 0) {} fence(); };
  auto slip = [&]() INL { T += SLOT; asm volatile("ds_max_u32 %0, %1\n s_waitcnt lgkmcnt(0)" :: "v"(L + L_T + 4), "v"(T - T0) : "memory"); };
  auto sync = [&]() INL { fence(); T = T0 + __builtin_amdgcn_readfirstlane(lrd<unsigned>(L + L_T + 4)); fence(); };
  auto rowPoll = [&](int c, unsigned off, unsigned box, unsigned img, int cn) INL {
    int it = zs();
    v4u v;
    do {
      if (it) { slip(); }
      ++it;
      until(c);
      v = ld4<SC1>(rW, off, box);
      lwr<v4u>(img, v);
      SB();
    } while (RARE(anyl(mx4(v) == ~0u && it < CAP)));
  };
  auto opath = [&](auto half) INL {
    constexpr bool lo = decltype(half)::v;
    front(true, lo);
    zeroZ();
    pv(O0);
    v8u wf[6];
    const unsigned pairBox = WS_BOX + ((r & ~1) * 8 + w) * BOX;
    unsigned aML = L + L_OI + (((2 * g + i) & 7) + (i & 8)) * 448 + 384; pin(aML);
    unsigned oXo = (lane & 55) == 0 ? 32 * (w & 3) + 2 * i : OOB; pin(oXo);
    unsigned oMg = g * 8 * BOX + 16 * i; pin(oMg);
    unsigned oGo = (g >> 1) * 8 * BOX + 16 * (lane & 31); pin(oGo);
    unsigned aGo = L + L_OI + (8 * (g >> 1) + w) * 448 + sel(u < 6, 96 * (qd & 3) + 16 * u, (lane & 31) == 6 ? 384 : 416); pin(aGo);
    unsigned aWo = L + L_KS + (8 * g + tq) * 288 + 32 * (w & 3) + 8 * tp; pin(aWo);
    auto phaseO = [&](int XO, v4f d, v4f e) INL {
      if (lo) return;
      v2f ml = lrd<v2f>(aML);
      v4u a[6];
#pragma unroll
      for (int c = 0; c < 6; ++c) {
        a[c] = lrd<v4u>(aA + L_OI + 128 * (c >> 1) + 8 * 448 * (c & 1));
        if (XO == D0 && (c & 1)) wf[c] = trd16(aWo + 64 * 288 * (c >> 1) + 128, 288);
      }
      SB();
      const float m = max8(ml.x);
      SB();
      d = smf<0>(a[0], wf[0], d, idx);
      SB();
      float f = __builtin_amdgcn_exp2f(ml.x - m);
      e = smf<0>(a[1], wf[1], e, idx);
      SB();
      float sm = f * ml.y;
      sm += dpp<X1P>(sm);
      d = smf<0>(a[2], wf[2], d, idx);
      SB();
      sm += dpp<X2P>(sm);
      e = smf<0>(a[3], wf[3], e, idx);
      SB();
      sm += dpp<HMIR>(sm);
      d = smf<0>(a[4], wf[4], d, idx);
      SB();
      sm = __builtin_amdgcn_rcpf(sm);
      e = smf<0>(a[5], wf[5], e, idx);
      SB();
      f *= sm;
      const v2f fa = {dpp<BCAST + 0>(f), dpp<BCAST + 1>(f)}, fb = {dpp<BCAST + 8>(f), dpp<BCAST + 9>(f)};
      SB();
      v2f p = v2f{d[0], d[1]} * fa.x;
      p += v2f{d[2], d[3]} * fa.y;
      p += v2f{e[0], e[1]} * fb.x;
      p += v2f{e[2], e[3]} * fb.y;
      const float v = add32(add16(p.x + p.y));
      st4r(rW, oXo + XO, myBox, col4<2>(canon(pk(v, dpp<X1P>(v)))));
      fence();
    };
    auto gatherM = [&](int c, int DO, int cn) INL {
      const unsigned base = WS_BOX + (32 * w + 5) * BOX + DO + 256 * s;
      int it = zs();
      v4u a[3];
      v4f d;
      do {
        if (it) { if (!anyl(max(max(mx4(a[0]), mx4(a[1])), mx4(a[2])) == ~0u)) it = CAP; slip(); }
        ++it;
        until(c);
#pragma unroll
        for (int q = 0; q < 3; ++q) a[q] = ld4<SC1>(rW, oMg, base + q * BOX);
        d = zero4();
        SB();
        d = smf<1>(del, cat(a[0], a[1]), d, idx);
        d = smf<1>(del, cat(a[2], v4u{0, 0, 0, 0}), d, idx);
        lwr<v2u>(aXSw, v2u{pk(d[0], d[1]), pk(d[2], d[3])});
        SB();
      } while (RARE(anyl(__builtin_isunordered(d[0], d[2]) && it < CAP)));
      bar();
    };
    auto stWo = [&](int j, v4u x) INL { const int row = ((tid >> 3) & 31) + 32 * (j % 6);
      const int d = j % 6 < 4 ? 16 * ((row >> 1) & 3) + (row >> 3) : 64 + 16 * ((row >> 1) & 1) + ((row - 128) >> 2);
      lwr<v4u>(L + L_KS + (2 * kpair(d) + (row & 1)) * 288 + 128 * (j / 6) + 16 * u, x); };
    v4f d0 = zero4(), d1 = zero4();
    v16u py;
    if (lo) {
#pragma unroll
      for (int j = 0; j < 6; ++j) stWo(j, tw[j]);
      bar();
      readWq();
      fence();
      int it = CAP;
      unsigned m;
#define PL(r, q) "buffer_load_dwordx4 " r ", %3, %4, %5 offen offset:512 sc1\n buffer_load_dwordx4 " q ", %3, %4, %6 offen offset:512 sc1\n"
#define PC(r, q, a, b, c, d, e, f, g, h) "s_waitcnt vmcnt(2)\n ds_write_b128 %7, " r "\n ds_write_b128 %7, " q " offset:1792\n v_max_u32 %1, " a ", " b "\n v_max3_u32 %1, " c ", " d ", %1\n" \
      "v_max3_u32 %1, " e ", " f ", %1\n v_max3_u32 %1, " g ", " h ", %1\n v_cmp_eq_u32 vcc, -1, %1\n s_cbranch_vccz 2f\n" "s_waitcnt lgkmcnt(0)\n"
      asm volatile("s_sleep %9\n" PL("v[240:243]", "v[244:247]") "1:\n s_sleep %8\n" PL("v[248:251]", "v[252:255]")
        PC("v[240:243]", "v[244:247]", "v240", "v241", "v242", "v243", "v244", "v245", "v246", "v247")
        "s_sleep %8\n" PL("v[240:243]", "v[244:247]")
        PC("v[248:251]", "v[252:255]", "v248", "v249", "v250", "v251", "v252", "v253", "v254", "v255")
        "s_sub_u32 %0, %0, 1\n s_cbranch_scc0 1b\n2:\n s_mov_b64 exec, -1\n"
        : "+s"(it), "=&v"(m), "={v[240:255]}"(py) : "v"(oGo), "s"(rW), "s"(pairBox), "s"(pairBox + 4 * BOX), "v"(aGo), "n"(SL_P), "n"(SL_O0) : "vcc", "scc", "memory");
    } else {
      bar();
#pragma unroll
      for (int c = 0; c < 6; c += 2) wf[c] = trd16(aWo + 64 * 288 * (c >> 1), 288);
      fence();
#pragma unroll
      for (int j = 6; j < 12; ++j) stWo(j, tw[j - 6]);
    }
    bar();
    phaseO(D0, d0, d1);
    if (lo) asm volatile("s_waitcnt vmcnt(0)" : "+{v[240:255]}"(py));
    if (!lo) readWq();
    d0 = zero4(); d1 = zero4();
    waitT(23);
    if (!lo) st2r(rW, sel(b == 0 && tid == 448, 0), 0, v2u{tagL, tagH});
    bar();
    readT();
    gatherM(C_M0, D0, 27);
    phaseQ(1, d0, d1);
    d0 = zero4(); d1 = zero4(); zeroZ();
    sync();
    if (w < 5) rowPoll(C_Q, oG + Q1, prtBox, aGw + L_QI, 28);
    bar();
    qk(d0, d1);
    pv(O1);
    d0 = zero4(); d1 = zero4();
    sync();
    rowPoll(C_O, oGo + O1, pairBox, aGo, 29);
    bar();
    phaseO(D1, d0, d1);
    if (isF) {
      d0 = zero4();
      sync();
      gatherM(C_M1, D1, 31);
      phaseQ(2, d0, d0);
    }
    __builtin_amdgcn_endpgm();
  };
  auto bpath = [&](auto half) INL {
    constexpr bool lo = decltype(half)::v;
    front(false, lo);
    loadW1(NB, 4);
    fence();
    zeroZ();
    pv(O0);
    v8u wf[4];
    float cs1, bias;
    v4f xr = {lo16(xin.x), hi16(xin.x), lo16(xin.y), hi16(xin.y)};
    unsigned oXg = (16 * g + 2 * w + (i >> 3) + (2 * w + (i >> 3) < 5 ? 0 : 3)) * BOX + 16 * u; pin(oXg);
    unsigned aXPr = L + L_XS + w * 256 + 32 * g + 16 * (i & 1); pin(aXPr);
    unsigned aHPw = L + L_HP + 32 * i + 4 * w; pin(aHPw);
    unsigned aS8w = L + L_S8 + 8 * w + 4 * (g >> 1); pin(aS8w);
    unsigned aHPr = L + L_HP + 32 * (8 * (g & 1) + u); pin(aHPr);
    unsigned mk = (g >> 1) == ((i >> 1) & 1) ? ~0u : 0u; pin(mk);
    unsigned oM = g == 0 ? 256 * w + 16 * i : OOB; pin(oM);
    auto permW2 = [&]() INL {
#pragma unroll
      for (int c = 0; c < 2; ++c)
#pragma unroll
        for (int hf = 0; hf < 2; ++hf)
#pragma unroll
          for (int k = 0; k < 2; ++k) {
            wf[2 + c][4 * hf + 2 * k] = __builtin_amdgcn_perm(t[1 + 2 * k][2 * c + hf], t[2 * k][2 * c + hf], 0x05040100u);
            wf[2 + c][4 * hf + 2 * k + 1] = __builtin_amdgcn_perm(t[1 + 2 * k][2 * c + hf], t[2 * k][2 * c + hf], 0x07060302u);
          }
      pin(wf[2]); pin(wf[3]);
      SB();
    };
    auto phaseB = [&](int c, int DO, int cn) INL {
      v4f dA, dB;
      if (w < 5) {
                v4f h, d;
        v4u x0, x1;
        int it = zs();
        v4u a[4];
        do {
          if (it) {
            unsigned m = 0;
#pragma unroll
            for (int q = 0; q < 4; ++q) m = max(m, mx4(a[q]));
            if (!anyl(m == ~0u)) it = CAP;
            slip();
          }
          ++it;
          until(c);
#pragma unroll
          for (int q = 0; q < 4; ++q) a[q] = ld4<SC1>(rW, oXg, WS_BOX + DO + 64 * q * BOX);
          h = zero4();
          d = xr; pin(d);
          SB();
#pragma unroll
          for (int q = 0; q < 2; ++q) d = smf<1>(del, cat(a[2 * q], a[2 * q + 1]), d, idx);
          lwr<v2u>(aXSw, v2u{pk(d[0], d[1]), pk(d[2], d[3])});
          asm volatile("" ::: "memory");
          x0 = lrd<v4u>(aXPr); x1 = lrd<v4u>(aXPr + 128);
          SB();
        } while (RARE(anyl(__builtin_isunordered(d[0], d[2]) && it < CAP)));
        float sx = fold(d), sxx = fold(d * d);
        float sv = add16(sum16(g >= 2 ? sx : sxx));
        lwr<float>(aS8w, sv);
        SB();
        h = smf<0>(x1, wf[1], smf<0>(x0, wf[0], h, idx), idx);
        SB();
        d *= 1.f / 96;
#pragma unroll
        for (int k = 0; k < 4; ++k) { auto sw = __builtin_amdgcn_permlane16_swap(__float_as_uint(d[k]), __float_as_uint(d[k]), false, false); dA[k] = __uint_as_float(sw[0]); dB[k] = __uint_as_float(sw[1]); }
        SB();
        lwr<float>(aHPw, h[0] + h[1]);
      }
      bar();
      if (w < 5) {
        v4f s0 = lrd<v4f>(L + L_S8), s1 = lrd<v4f>(L + L_S8 + 16); v2f s2 = lrd<v2f>(L + L_S8 + 32);
        v4f hp = lrd<v4f>(aHPr); float h4 = lrd<float>(aHPr + 16);
        SB();
        if (cn == 26) permW2();
        v2f tt = ((v2f{s0[0], s0[1]} + v2f{s0[2], s0[3]}) + (v2f{s1[0], s1[1]} + v2f{s1[2], s1[3]}) + s2) * (1.f / 640);
        float hr = fold(hp) + h4;
        float mean = tt.y, var = fmaxf(tt.x - mean * mean, 0.f);
        float hh = (hr - mean * cs1) * __builtin_amdgcn_rsqf(var + 1e-5f) + bias;
        float e = __builtin_amdgcn_exp2f(fminf(hh * 1.4426950408889634f, 50.f));
        float n = e * (e + 2.f);
        float act = (hh * n) * __builtin_amdgcn_rcpf(n + 2.f);
        unsigned pp = pk(act, dpp<X1P>(act));
        v4u bc = {dppu<BCAST + 0>(pp) & mk, dppu<BCAST + 2>(pp) & mk, dppu<BCAST + 4>(pp) & mk, dppu<BCAST + 6>(pp) & mk};
        dA = smf<0>(bc, wf[2], dA, idx);
        dB = smf<0>(bc, wf[3], dB, idx);
        st4r(rW, oM + DO, myBox, v4u{canon(pk(dA[0], dA[1])), canon(pk(dA[2], dA[3])), canon(pk(dB[0], dB[1])), canon(pk(dB[2], dB[3]))});
      }
      fence();
    };
    if (w < 5) {
      const unsigned a1w = L + L_WQ + w * 4096 + (2 * kpair(lane >> 2) + ((lane >> 1) & 1)) * 32 + 16 * (lane & 1);
      bias = lo16(__builtin_amdgcn_raw_buffer_load_b16(mkrs(b1, 3072), 2 * (8 * (g & 1) + u), 32 * pB, 0));
      loadW2();
      fence();
#pragma unroll
      for (int j = 0; j < 4; ++j) lwr<v4u>(a1w + 1024 * j, tw[j]);
      asm volatile("" ::: "memory");
#pragma unroll
      for (int c = 0; c < 2; ++c) wf[c] = trd16(L + L_WQ + w * 4096 + (64 * c + 8 * g + tq) * 32 + 8 * tp, 32);
      const v4u on = {0x3f803f80u, 0x3f803f80u, 0x3f803f80u, 0x3f803f80u};
      v4f h = smf<0>(on, wf[1], smf<0>(on, wf[0], z4, idx), idx);
      lwr<float>(aHPw + 512, h[0] + h[1]);
    }
    if (w == 5) {
      const rsrc_t rc = mkrs((const void*)pc0, CODELEN);
      unsigned m = 0;
#pragma unroll
      for (int j = 0; j < (CODELEN + 8191) / 8192; ++j) m |= ld1<0>(rc, sel(lane * 128 + 8192 * j < CODELEN, lane * 128 + 8192 * j));
      pin(m);
    }
    waitT(23);
    bar();
    cs1 = fold(lrd<v4f>(aHPr + 512)) + lrd<float>(aHPr + 528);
    readT();
    phaseB(C_X0, D0, 26);
    sync();
    v4f d0 = zero4(), d1 = zero4(); zeroZ();
    if (w < 5) rowPoll(C_Q, oG + Q1, prtBox, aGw + L_QI, 28);
    bar();
    qk(d0, d1);
    pv(O1);
    if (w < 5) {
      v4u v = ld4<SC1>(rW, 32 * i + 16 * (g & 1), prtBox + XR2);
      sync();
      if (RARE(anyl(mx4(v) == ~0u))) {
        int it = 0;
        do { fence(); v = ld4<SC1>(rW, 32 * i + 16 * (g & 1), prtBox + XR2); } while (anyl(mx4(v) == ~0u) && ++it < CAP);
      }
      xr = __builtin_bit_cast(v4f, v);
    }
    phaseB(C_X, D1, 30);
    __builtin_amdgcn_endpgm();
  };
  if (!isB) {
    if (w < 4) opath(Half<true>{}); else opath(Half<false>{});
  } else {
    if (w < 4) bpath(Half<true>{}); else bpath(Half<false>{});
  }
}

extern "C" void kernel_launch(void* const* d_in, const int* in_sizes, int n_in, void* d_out, int out_size, void* d_ws, size_t ws_size, hipStream_t stream) {
  k_fused<<<256, 512, 0, stream>>>(d_in[0], d_in[1], d_in[2], d_in[3], d_in[4], d_in[6], d_in[7], d_in[8], d_out, (char*)d_ws);
}
```
